```python
import torch

def _softmax(x, axis=-1):
    return torch.softmax(x, dim=axis)
def _gelu(x):
    return torch.nn.functional.gelu(x, approximate="tanh")
def _silu(x):
    return torch.nn.functional.silu(x)
def _sigmoid(x):
    return torch.sigmoid(x)
def _layernorm(x, axis=-1, eps=1e-5):
    m = x.mean(dim=axis, keepdim=True)
    v = x.var(dim=axis, keepdim=True, unbiased=False)
    return (x - m) / torch.sqrt(v + eps)
def _scatter_add(tgt, idx, src, axis=0):
    return tgt.clone().index_add_(axis, idx, src)
def _segment_sum(src, seg, n):
    out = torch.zeros((n,) + src.shape[1:], dtype=src.dtype)
    return out.index_add_(0, seg, src)
def _pad(x, pw):
    flat = tuple(v for pair in reversed(pw) for v in pair)
    return torch.nn.functional.pad(x, flat)

kernel_name = 'recurrent_scan__ssm_conv_s4d__moe_arch_mod__prompt'

def setup_inputs():

    _w = (lambda a: (lambda t: t.to(torch.bfloat16) if t.is_floating_point() else t)(torch.as_tensor(a)))
    def _A_setup():
        Bsz, L, D, N = 1, 2048, 512, 128
        return {
            "u": _w((lambda *s: torch.randn(*s, dtype=torch.bfloat16) * 0.05)(Bsz, L, D)),
            "log_A_real": _w((lambda *s: torch.randn(*s, dtype=torch.bfloat16) * 3.2)(D, N) * 0.5),
            "A_imag": _w((lambda *s: torch.randn(*s, dtype=torch.bfloat16) * 0.05)(D, N) * 3.14),
            "C_real": _w((lambda *s: torch.randn(*s, dtype=torch.bfloat16) * 0.1)(D, N) / N ** 0.5),
            "C_imag": _w((lambda *s: torch.randn(*s, dtype=torch.bfloat16) * 0.1)(D, N) / N ** 0.5),
            "log_dt": _w((lambda *s: torch.randn(*s, dtype=torch.bfloat16) * 0.05)(D) * 0.5 - 4.0),
            "D_skip": _w((lambda *s: torch.randn(*s, dtype=torch.bfloat16) * 0.05)(D)),
        }
    _A_in = _A_setup()

    def _B_setup():
        B, S, D, F, C = 1, 2048, 512, 1376, 2
        return {
            "x": _w((lambda *s: torch.randn(*s, dtype=torch.bfloat16) * 0.05)(B, S, D)),
            "w_r": _w((lambda *s: torch.randn(*s, dtype=torch.bfloat16) * 3.2)(D)),
            "w_gate": _w((lambda *s: torch.randn(*s, dtype=torch.bfloat16) * 0.1)(D, F)),
            "w_up": _w((lambda *s: torch.randn(*s, dtype=torch.bfloat16) * 0.1)(D, F)),
            "w_down": _w((lambda *s: torch.randn(*s, dtype=torch.bfloat16) * 0.1)(F, D)),
        }
    _B_in = _B_setup()

    return {
        "A_u": _A_in["u"],
        "A_log_A_real": _A_in["log_A_real"],
        "A_A_imag": _A_in["A_imag"],
        "A_C_real": _A_in["C_real"],
        "A_C_imag": _A_in["C_imag"],
        "A_log_dt": _A_in["log_dt"],
        "A_D_skip": _A_in["D_skip"],
        "B_x": _B_in["x"],
        "B_w_r": _B_in["w_r"],
        "B_w_gate": _B_in["w_gate"],
        "B_w_up": _B_in["w_up"],
        "B_w_down": _B_in["w_down"],
    }

def reference(A_u, A_log_A_real, A_A_imag, A_C_real, A_C_imag, A_log_dt, A_D_skip, B_x, B_w_r, B_w_gate, B_w_up, B_w_down):
    def _A_ref(u, log_A_real, A_imag, C_real, C_imag, log_dt, D_skip):
        L = u.shape[1]
        dt = torch.exp(log_dt)
        A = -torch.exp(log_A_real) + 1j * A_imag
        C = (C_real + 1j * C_imag) * dt[:, None]
        pos = (lambda x, dt: x.to(dt))(torch.arange(L), log_dt.dtype)
        P = torch.exp((A * dt[:, None])[:, :, None] * pos[None, None, :])
        K = 2.0 * torch.einsum("dn,dnl->dl", C, P).real
        L2 = 2 * L
        u_t = (lambda x, *d: x.permute(*d))(u, 0, 2, 1)
        Kf = (lambda x, *a, **k: torch.fft.rfft(x.float(), *a, **k))(K, L2)
        Uf = (lambda x, *a, **k: torch.fft.rfft(x.float(), *a, **k))(u_t, L2)
        y = (lambda x, *a, **k: torch.fft.irfft(x, *a, **k).to(torch.bfloat16))(Uf * Kf[None, :, :], L2)[:, :, :L]
        y = (lambda x, *d: x.permute(*d))(y, 0, 2, 1)
        return y + u * D_skip[None, None, :]

    def _B_ref(x, w_r, w_gate, w_up, w_down):
        B, S, D = x.shape
        C = 2
        r = x @ w_r
        idx = torch.argsort(-r, axis=-1)[:, :C]
        idx_e = (lambda x, n, axis: torch.repeat_interleave(x, n, dim=axis))(idx.reshape(B, C, 1), D, axis=2)
        x_sel = (lambda x, i, axis: torch.take_along_dim(x, i, dim=axis))(x, idx_e, axis=1)
        g = _silu(x_sel @ w_gate)
        u = x_sel @ w_up
        y = (g * u) @ w_down
        r_sel = _sigmoid((lambda x, i, axis: torch.take_along_dim(x, i, dim=axis))(r, idx, axis=1))
        y = y * r_sel.reshape(B, C, 1)
        b_off = (torch.arange(B) * S).reshape(B, 1)
        flat_idx = (idx + b_off).reshape(B * C)
        zero = (x * 0.0).reshape(B * S, D)
        upd = _scatter_add(zero, flat_idx, y.reshape(B * C, D), axis=0)
        return x + upd.reshape(B, S, D)

    x = A_u
    xn = _layernorm(x, axis=-1)
    s = _A_ref(xn, A_log_A_real, A_A_imag, A_C_real, A_C_imag, A_log_dt, A_D_skip)
    g = _silu(_B_ref(xn, B_w_r, B_w_gate, B_w_up, B_w_down))
    return x + s * g
```

```cpp
#include <hip/hip_runtime.h>
#include <hip/hip_bf16.h>
#include <stdint.h>
typedef unsigned short u16;
#define L_ 2048
#define D_ 512
#define N_ 128
#define F_ 1376
__device__ __forceinline__ float bf2f(u16 v){ return __uint_as_float(((unsigned)v)<<16); }
__device__ __forceinline__ u16 f2bf(float f){ unsigned u=__float_as_uint(f); return (u16)((u + 0x7FFF + ((u>>16)&1))>>16); }
__device__ __forceinline__ float rbf(float f){ return bf2f(f2bf(f)); }

__global__ void k_ln(const u16* u, const u16* wr, u16* xn, float* r){
  int tok = blockIdx.x; int lane = threadIdx.x;
  float x[8]; float s=0;
  for(int i=0;i<8;i++){ x[i]=bf2f(u[tok*D_+lane*8+i]); s+=x[i]; }
  for(int o=32;o;o>>=1) s+=__shfl_xor(s,o);
  float mean=s/D_; float v=0;
  for(int i=0;i<8;i++){ float d=x[i]-mean; v+=d*d; }
  for(int o=32;o;o>>=1) v+=__shfl_xor(v,o);
  float m=rbf(mean); float var=rbf(v/D_);
  float sd=rbf(sqrtf(rbf(var+1e-5f)));
  float acc=0;
  for(int i=0;i<8;i++){ float t=rbf(rbf(x[i]-m)/sd); xn[tok*D_+lane*8+i]=f2bf(t); acc+=t*bf2f(wr[lane*8+i]); }
  for(int o=32;o;o>>=1) acc+=__shfl_xor(acc,o);
  if(lane==0) r[tok]=rbf(acc);
}
__global__ void k_kgen(const u16* logA, const u16* Aim, const u16* Cre, const u16* Cim, const u16* logdt, float* K){
  int d=blockIdx.y; int l=blockIdx.x*blockDim.x+threadIdx.x;
  float pos=rbf((float)l);
  float dt=rbf(expf(bf2f(logdt[d])));
  float acc=0;
  for(int n=0;n<N_;n++){
    float ar=-rbf(expf(bf2f(logA[d*N_+n])))*dt; float ai=bf2f(Aim[d*N_+n])*dt;
    float cr=bf2f(Cre[d*N_+n])*dt, ci=bf2f(Cim[d*N_+n])*dt;
    float e=expf(ar*pos); float sn,cs; sincosf(ai*pos,&sn,&cs);
    acc+= e*(cr*cs-ci*sn);
  }
  K[d*L_+l]=2.f*acc;
}
__global__ void k_top(const float* r, int* idx){
  if(threadIdx.x==0){ int b0=0,b1=-1; float v0=r[0], v1=-INFINITY;
    for(int i=1;i<L_;i++){ float v=r[i]; if(v>v0){v1=v0;b1=b0;v0=v;b0=i;} else if(v>v1){v1=v;b1=i;} }
    idx[0]=b0; idx[1]=b1; }
}
__global__ void k_gu(const u16* xn, const int* idx, const u16* wg, const u16* wu, float* h){
  int f=blockIdx.x*blockDim.x+threadIdx.x; int c=blockIdx.y; if(f>=F_) return;
  int tok=idx[c]; float g=0,u=0;
  for(int d=0;d<D_;d++){ float x=bf2f(xn[tok*D_+d]); g+=x*bf2f(wg[d*F_+f]); u+=x*bf2f(wu[d*F_+f]); }
  g=rbf(g); u=rbf(u); g=rbf(g/(1.f+expf(-g)));
  h[c*F_+f]=rbf(g*u);
}
__global__ void k_down(const float* h, const int* idx, const float* r, const u16* wd, float* y){
  int d=blockIdx.x*blockDim.x+threadIdx.x; int c=blockIdx.y;
  float a=0; for(int f=0;f<F_;f++) a+=h[c*F_+f]*bf2f(wd[f*D_+d]);
  a=rbf(a); float rs=rbf(1.f/(1.f+expf(-r[idx[c]])));
  y[c*D_+d]=rbf(a*rs);
}
__global__ void k_conv(const u16* u, const u16* xn, const float* K, const u16* Dsk, const int* idx, const float* y, u16* out){
  int d=blockIdx.y; int l=blockIdx.x*blockDim.x+threadIdx.x;
  __shared__ float xs[L_]; __shared__ float ks[L_];
  for(int i=threadIdx.x;i<L_;i+=blockDim.x){ xs[i]=bf2f(xn[i*D_+d]); ks[i]=K[d*L_+i]; }
  __syncthreads();
  float a=0; for(int j=0;j<=l;j++) a+=ks[j]*xs[l-j];
  float xv=xs[l];
  float s=rbf(rbf(a)+rbf(xv*bf2f(Dsk[d])));
  float b=xv; if(l==idx[0]) b=rbf(xv+y[d]); if(l==idx[1]) b=rbf(xv+y[D_+d]);
  float g=rbf(b/(1.f+expf(-b)));
  out[l*D_+d]=f2bf(bf2f(u[l*D_+d])+rbf(s*g));
}
extern "C" void kernel_launch(void* const* d_in, const int* in_sizes, int n_in, void* d_out, int out_size, void* d_ws, size_t ws_size, hipStream_t stream){
  const u16* u=(const u16*)d_in[0];
  char* ws=(char*)d_ws;
  u16* xn=(u16*)ws; float* r=(float*)(ws+(2<<20)); int* idx=(int*)(ws+(2<<20)+16384); float* h=(float*)(ws+(2<<20)+32768); float* y=(float*)(ws+(2<<20)+65536); float* K=(float*)(ws+(4<<20));
  k_ln<<<L_,64,0,stream>>>(u,(const u16*)d_in[8],xn,r);
  k_kgen<<<dim3(L_/256,D_),256,0,stream>>>((const u16*)d_in[1],(const u16*)d_in[2],(const u16*)d_in[3],(const u16*)d_in[4],(const u16*)d_in[5],K);
  k_top<<<1,64,0,stream>>>(r,idx);
  k_gu<<<dim3((F_+63)/64,2),64,0,stream>>>(xn,idx,(const u16*)d_in[9],(const u16*)d_in[10],h);
  k_down<<<dim3(D_/64,2),64,0,stream>>>(h,idx,r,(const u16*)d_in[11],y);
  k_conv<<<dim3(L_/256,D_),256,0,stream>>>(u,xn,K,(const u16*)d_in[6],idx,y,(u16*)d_out);
}
```

```cpp
#include <hip/hip_runtime.h>

__global__ __launch_bounds__(512, 2) void s4d_moe_kernel(
    const void* u, const void* logA, const void* Aim, const void* Cre, const void* Cim, const void* logdt,
    const void* Dsk, const void* wr, const void* wg, const void* wu, const void* wd, void* out, void* ws) {
#if defined(__HIP_DEVICE_COMPILE__)
    asm volatile(
        "L_START:\n"
        "s_load_dwordx16 s[4:19], s[0:1], 0x0\n"
        "s_load_dwordx8 s[20:27], s[0:1], 0x40\n"
        "s_load_dwordx2 s[28:29], s[0:1], 0x60\n"
        "s_getpc_b64 s[36:37]\n"
        "v_readfirstlane_b32 s30, v0\n"
        "v_and_b32 v1, 63, v0\n"
        "s_bfe_u32 s35, s2, 0x20003\n"
        "s_and_b32 s31, s2, 7\n"
        "s_lshr_b32 s30, s30, 6\n"
        "s_lshl3_add_u32 s35, s35, s30\n"
        "s_mul_i32 s35, s35, 640\n"
        "s_lshr_b32 s32, s2, 4\n"
        "v_lshl_add_u32 v2, v1, 7, s35\n"
        "s_and_b32 s36, s36, 0xffffff00\n"
        "s_mov_b64 exec, 31\n"
        "global_load_dword v252, v2, s[36:37]\n"
        "s_mov_b64 exec, -1\n"
        "s_lshl4_add_u32 s33, s31, s32\n"
        "s_and_b32 s34, s30, 3\n"
        "s_bitcmp1_b32 s2, 3\n"
        "s_cbranch_scc1 L_AUX\n"
        "s_cmp_ge_u32 s30, 4\n"
        "s_cbranch_scc1 L_E\n"
        "s_lshl2_add_u32 s38, s33, s34\n"
        "s_lshl_b32 s38, s38, 8\n"
        "v_lshl_add_u32 v3, v1, 2, s38\n"
        "s_lshl3_add_u32 s35, s32, s30\n"
        "s_mul_i32 s38, s35, 12288\n"
        "s_lshl_b32 s39, s31, 7\n"
        "s_add_u32 s38, s38, s39\n"
        "v_lshl_add_u32 v7, v1, 10, s38\n"
        "s_lshl_b32 s39, s33, 3\n"
        "s_waitcnt lgkmcnt(0)\n"
        "global_load_dword v244, v3, s[6:7]\n"
        "global_load_dword v245, v3, s[8:9]\n"
        "global_load_dword v246, v3, s[10:11]\n"
        "global_load_dword v247, v3, s[12:13]\n"
        "s_mov_b64 exec, 0xfff\n"
        "global_load_dword v253, v7, s[4:5]\n"
        "s_load_dwordx2 s[62:63], s[14:15], s39\n"
        "s_load_dwordx2 s[42:43], s[16:17], s39\n"
        "s_load_dword s40, s[28:29], 0x0\n"
        "s_mov_b64 exec, -1\n"
        "s_barrier\n"
        "v_and_b32 v2, 15, v1\n"
        "v_lshrrev_b32_e64 v3, 4, v1\n"
        "s_mul_i32 s38, s30, 4608\n"
        "s_mov_b32 m0, s38\n"
        "s_mul_i32 s54, s34, 3328\n"
        "s_add_u32 s54, s54, 36864\n"
        "s_mul_i32 s55, s34, 4608\n"
        "s_add_u32 s55, s55, 50176\n"
        "v_mov_b32 v46, 0\n"
        "v_mov_b32 v47, 0\n"
        "v_mov_b64 v[48:49], v[46:47]\n"
        "v_lshl_add_u32 v4, v1, 4, s54\n"
        "ds_write_b128 v4, v[46:49]\n"
        "ds_write_b128 v4, v[46:49] offset:1024\n"
        "ds_write_b128 v4, v[46:49] offset:2048\n"
        "ds_write_b128 v4, v[46:49] offset:2304\n"
        "v_lshl_add_u32 v4, v1, 3, s55\n"
        "ds_write_b64 v4, v[46:47]\n"
        "s_lshl_b32 s39, s34, 9\n"
        "s_add_u32 s39, s39, 109568\n"
        "v_lshl_add_u32 v4, v1, 3, s39\n"
        "ds_write_b64 v4, v[46:47]\n"
        "s_mov_b32 s39, 109568\n"
        "v_lshl_add_u32 v23, v1, 2, s39\n"
        "v_mov_b32 v254, 1\n"
        "v_and_b32 v5, 48, v1\n"
        "v_mul_u32_u24 v4, 288, v2\n"
        "v_add3_u32 v10, v4, v5, s38\n"
        "s_mul_i32 s39, s34, 3072\n"
        "s_add_u32 s39, s39, 68608\n"
        "v_lshl_add_u32 v11, v1, 4, s39\n"
        "v_lshlrev_b32 v6, 4, v2\n"
        "v_and_b32 v4, 32, v1\n"
        "v_lshl_add_u32 v4, v4, 3, v6\n"
        "v_and_b32 v7, 16, v1\n"
        "v_lshrrev_b32 v7, 1, v7\n"
        "v_add_u32 v4, v4, v7\n"
        "v_sub_u32 v12, s54, v4\n"
        "s_sub_u32 s39, s54, 512\n"
        "v_lshlrev_b32 v4, 4, v1\n"
        "v_sub_u32 v13, s39, v4\n"
        "v_lshlrev_b32_e64 v4, 7, v3\n"
        "v_lshl_add_u32 v7, v2, 1, v4\n"
        "v_lshl_add_u32 v8, v2, 2, v4\n"
        "s_add_u32 s39, s54, 3038\n"
        "v_sub_u32 v14, s39, v7\n"
        "s_add_u32 s39, s54, 2940\n"
        "s_mov_b32 exec_lo, 0\n"
        "v_sub_u32 v14, s39, v8\n"
        "s_mov_b32 exec_lo, -1\n"
        "s_add_u32 s39, s54, 1086\n"
        "v_mov_b32 v15, s39\n"
        "s_add_u32 s39, s54, 572\n"
        "v_writelane_b32 v15, s39, 63\n"
        "s_add_u32 s39, s54, 58\n"
        "v_writelane_b32 v15, s39, 31\n"
        "v_and_b32 v4, 1, v1\n"
        "v_mul_u32_u24 v4, 1218, v4\n"
        "v_lshl_add_u32 v4, v2, 1, v4\n"
        "v_sub_u32 v4, v5, v4\n"
        "s_add_u32 s39, s54, 1312\n"
        "v_add_u32 v16, s39, v4\n"
        "v_subrev_u32 v17, 64, v16\n"
        "v_lshlrev_b32 v4, 3, v2\n"
        "v_sub_u32 v4, v5, v4\n"
        "s_add_u32 s39, s54, 2488\n"
        "v_add_u32 v18, s39, v4\n"
        "v_lshlrev_b32 v4, 2, v2\n"
        "v_sub_u32 v4, v5, v4\n"
        "s_add_u32 s39, s54, 2940\n"
        "v_add_u32 v19, s39, v4\n"
        "v_lshl_add_u32 v4, v2, 5, v5\n"
        "s_add_u32 s39, s55, 32\n"
        "v_add_u32 v20, s39, v4\n"
        "s_add_u32 s39, s55, 464\n"
        "v_add3_u32 v21, v6, v5, s39\n"
        "s_mul_i32 s39, s34, 10240\n"
        "s_add_u32 s39, s39, 68608\n"
        "v_mul_u32_u24 v4, 320, v3\n"
        "v_add3_u32 v22, v4, v6, s39\n"
        "v_lshrrev_b32 v4, 1, v5\n"
        "v_sub_u32 v4, v4, v2\n"
        "v_add_u32 v4, 16, v4\n"
        "v_min_u32 v4, 31, v4\n"
        "s_mov_b32 s39, 0x7c00000\n"
        "v_lshrrev_b32_e64 v7, v4, s39\n"
        "v_and_b32 v7, 0xff, v7\n"
        "v_lshl_or_b32 v7, v7, 15, v7\n"
        "v_and_b32 v26, 0x10001, v7\n"
        "v_mul_u32_u24 v26, 0x3c00, v26\n"
        "v_and_b32 v27, 0x40004, v7\n"
        "v_mul_u32_u24 v27, 0xf00, v27\n"
        "v_and_b32 v28, 0x100010, v7\n"
        "v_mul_u32_u24 v28, 0x3c0, v28\n"
        "v_and_b32 v29, 0x400040, v7\n"
        "v_mul_u32_u24 v29, 0xf0, v29\n"
        "s_mov_b32 s39, 0x38000000\n"
        "v_lshrrev_b32_e64 v7, v4, s39\n"
        "v_and_b32 v7, 0xff, v7\n"
        "v_lshl_or_b32 v7, v7, 15, v7\n"
        "v_and_b32 v30, 0x10001, v7\n"
        "v_mul_u32_u24 v30, 0x3c00, v30\n"
        "v_and_b32 v31, 0x40004, v7\n"
        "v_mul_u32_u24 v31, 0xf00, v31\n"
        "v_and_b32 v32, 0x100010, v7\n"
        "v_mul_u32_u24 v32, 0x3c0, v32\n"
        "v_and_b32 v33, 0x400040, v7\n"
        "v_mul_u32_u24 v33, 0xf0, v33\n"
        "s_mov_b32 s39, 0x1ff00000\n"
        "v_lshrrev_b32_e64 v7, v4, s39\n"
        "v_and_b32 v7, 0xff, v7\n"
        "v_lshl_or_b32 v7, v7, 15, v7\n"
        "v_and_b32 v34, 0x10001, v7\n"
        "v_mul_u32_u24 v34, 0x3c00, v34\n"
        "v_and_b32 v35, 0x40004, v7\n"
        "v_mul_u32_u24 v35, 0xf00, v35\n"
        "v_and_b32 v36, 0x100010, v7\n"
        "v_mul_u32_u24 v36, 0x3c0, v36\n"
        "v_and_b32 v37, 0x400040, v7\n"
        "v_mul_u32_u24 v37, 0xf0, v37\n"
        "s_mov_b32 s39, 0xfe00000\n"
        "v_lshrrev_b32_e64 v7, v4, s39\n"
        "v_and_b32 v7, 0xff, v7\n"
        "v_lshl_or_b32 v7, v7, 15, v7\n"
        "v_and_b32 v38, 0x10001, v7\n"
        "v_mul_u32_u24 v38, 0x3c00, v38\n"
        "v_and_b32 v39, 0x40004, v7\n"
        "v_mul_u32_u24 v39, 0xf00, v39\n"
        "v_and_b32 v40, 0x100010, v7\n"
        "v_mul_u32_u24 v40, 0x3c0, v40\n"
        "v_and_b32 v41, 0x400040, v7\n"
        "v_mul_u32_u24 v41, 0xf0, v41\n"
        "s_mov_b32 s39, 0x30000000\n"
        "v_lshrrev_b32_e64 v7, v4, s39\n"
        "v_and_b32 v7, 0xff, v7\n"
        "v_lshl_or_b32 v7, v7, 15, v7\n"
        "v_and_b32 v42, 0x10001, v7\n"
        "v_mul_u32_u24 v42, 0x3c00, v42\n"
        "v_and_b32 v43, 0x40004, v7\n"
        "v_mul_u32_u24 v43, 0xf00, v43\n"
        "v_and_b32 v44, 0x100010, v7\n"
        "v_mul_u32_u24 v44, 0x3c0, v44\n"
        "v_and_b32 v45, 0x400040, v7\n"
        "v_mul_u32_u24 v45, 0xf0, v45\n"
        "v_mov_b32 v248, 0x3fb8aa3b\n"
        "s_lshl_b32 s38, s34, 4\n"
        "s_waitcnt lgkmcnt(0)\n"
        "s_lshr_b64 s[62:63], s[62:63], s38\n"
        "s_lshl_b32 s62, s62, 16\n"
        "v_mul_f32 v46, s62, v248\n"
        "s_waitcnt vmcnt(4)\n"
        "v_lshlrev_b32 v47, 16, v244\n"
        "v_mul_f32 v47, v248, v47\n"
        "v_exp_f32 v46, v46\n"
        "v_exp_f32 v47, v47\n"
        "s_waitcnt vmcnt(2)\n"
        "v_lshlrev_b32 v56, 16, v246\n"
        "s_waitcnt vmcnt(1)\n"
        "v_lshlrev_b32 v57, 16, v247\n"
        "v_cvt_pk_bf16_f32 v46, v46, v47\n"
        "v_max3_f32 v60, |v56|, |v57|, |v246|\n"
        "v_lshlrev_b32_e64 v49, 16, v245\n"
        "v_max_f32_e64 v60, v60, |v247|\n"
        "v_and_b32 v48, 0xffff0000, v46\n"
        "v_lshlrev_b32_e64 v46, 16, v46\n"
        "v_max_u32_dpp v60, v60, v60 row_shr:1 row_mask:0xf bank_mask:0xf bound_ctrl:1\n"
        "v_mul_f32 v50, 0xbfb8aa3b, v46\n"
        "v_mul_f32 v51, 0.15915494, v46\n"
        "v_max_u32_dpp v60, v60, v60 row_shr:2 row_mask:0xf bank_mask:0xf bound_ctrl:1\n"
        "v_pk_mul_f32 v[52:53], v[48:49], v[50:51]\n"
        "v_readfirstlane_b32 s41, v46\n"
        "v_max_u32_dpp v60, v60, v60 row_shr:4 row_mask:0xf bank_mask:0xf bound_ctrl:1\n"
        "v_exp_f32 v90, v52\n"
        "v_fract_f32 v84, v53\n"
        "v_max_u32_dpp v60, v60, v60 row_shr:8 row_mask:0xf bank_mask:0xf bound_ctrl:1\n"
        "v_cos_f32 v62, v84\n"
        "v_sin_f32 v63, v84\n"
        "v_max_u32_dpp v60, v60, v60 row_bcast:15 row_mask:0xa bank_mask:0xf\n"
        "v_mul_f32 v54, 0x42800000, v52\n"
        "v_mul_f32 v55, 0x42800000, v53\n"
        "v_max_u32_dpp v60, v60, v60 row_bcast:31 row_mask:0xc bank_mask:0xf\n"
        "v_pk_mul_f32 v[62:63], v[62:63], v[90:91] op_sel_hi:[1,0]\n"
        "v_exp_f32_e64 v91, v54\n"
        "v_fract_f32_e64 v86, v55\n"
        "v_readlane_b32 s44, v60, 63\n"
        "v_pk_mul_f32 v[84:85], v[62:63], v[62:63] op_sel_hi:[1,0]\n"
        "v_cos_f32 v74, v86\n"
        "v_pk_fma_f32 v[64:65], v[62:63], v[62:63], v[84:85] op_sel:[1,1,0] op_sel_hi:[0,1,1] neg_lo:[0,1,0]\n"
        "v_sin_f32_e64 v75, v86\n"
        "s_lshr_b32 s44, s44, 23\n"
        "s_max_u32 s44, s44, 64\n"
        "s_min_u32 s44, s44, 190\n"
        "s_lshl_b32 s44, s44, 23\n"
        "s_sub_u32 s45, 0x7f000000, s44\n"
        "v_pk_mul_f32 v[84:85], v[64:65], v[64:65] op_sel_hi:[1,0]\n"
        "v_pk_mul_f32 v[74:75], v[74:75], v[90:91] op_sel:[0,1]\n"
        "v_pk_fma_f32 v[66:67], v[64:65], v[64:65], v[84:85] op_sel:[1,1,0] op_sel_hi:[0,1,1] neg_lo:[0,1,0]\n"
        "v_mul_f32_e64 v56, s45, v56\n"
        "v_mul_f32_e64 v57, s45, v57\n"
        "v_pk_mul_f32 v[84:85], v[66:67], v[66:67] op_sel_hi:[1,0]\n"
        "v_pk_mul_f32 v[86:87], v[74:75], v[74:75] op_sel_hi:[1,0]\n"
        "v_pk_mul_f32 v[88:89], v[56:57], v[62:63] op_sel_hi:[1,0]\n"
        "v_pk_fma_f32 v[68:69], v[66:67], v[66:67], v[84:85] op_sel:[1,1,0] op_sel_hi:[0,1,1] neg_lo:[0,1,0]\n"
        "v_pk_fma_f32 v[76:77], v[74:75], v[74:75], v[86:87] op_sel:[1,1,0] op_sel_hi:[0,1,1] neg_lo:[0,1,0]\n"
        "v_pk_fma_f32 v[92:93], v[56:57], v[62:63], v[88:89] op_sel:[1,1,0] op_sel_hi:[0,1,1] neg_lo:[0,1,0]\n"
        "v_pk_mul_f32 v[84:85], v[68:69], v[68:69] op_sel_hi:[1,0]\n"
        "v_pk_mul_f32 v[86:87], v[76:77], v[76:77] op_sel_hi:[1,0]\n"
        "v_pk_mul_f32 v[88:89], v[56:57], v[64:65] op_sel_hi:[1,0]\n"
        "v_pk_fma_f32 v[70:71], v[68:69], v[68:69], v[84:85] op_sel:[1,1,0] op_sel_hi:[0,1,1] neg_lo:[0,1,0]\n"
        "v_pk_fma_f32 v[78:79], v[76:77], v[76:77], v[86:87] op_sel:[1,1,0] op_sel_hi:[0,1,1] neg_lo:[0,1,0]\n"
        "v_pk_fma_f32 v[94:95], v[56:57], v[64:65], v[88:89] op_sel:[1,1,0] op_sel_hi:[0,1,1] neg_lo:[0,1,0]\n"
        "v_pk_mul_f32 v[84:85], v[70:71], v[70:71] op_sel_hi:[1,0]\n"
        "v_pk_mul_f32 v[86:87], v[68:69], v[70:71] op_sel_hi:[1,0]\n"
        "v_pk_mul_f32 v[88:89], v[56:57], v[66:67] op_sel_hi:[1,0]\n"
        "v_pk_fma_f32 v[72:73], v[70:71], v[70:71], v[84:85] op_sel:[1,1,0] op_sel_hi:[0,1,1] neg_lo:[0,1,0]\n"
        "v_pk_fma_f32 v[108:109], v[68:69], v[70:71], v[86:87] op_sel:[1,1,0] op_sel_hi:[0,1,1] neg_lo:[0,1,0]\n"
        "v_pk_fma_f32 v[98:99], v[56:57], v[66:67], v[88:89] op_sel:[1,1,0] op_sel_hi:[0,1,1] neg_lo:[0,1,0]\n"
        "v_mov_b32 v128, 0x3c00\n"
        "v_cvt_pk_f16_f32 v126, v74, -v75\n"
        "ds_write_addtid_b32 v128 offset:0\n"
        "ds_write_addtid_b32 v126 offset:2304\n"
        "v_pk_mul_f32 v[84:85], v[72:73], v[74:75] op_sel_hi:[1,0]\n"
        "v_pk_mul_f32 v[86:87], v[56:57], v[68:69] op_sel_hi:[1,0]\n"
        "v_pk_fma_f32 v[110:111], v[72:73], v[74:75], v[84:85] op_sel:[1,1,0] op_sel_hi:[0,1,1] neg_lo:[0,1,0]\n"
        "v_pk_fma_f32 v[106:107], v[56:57], v[68:69], v[86:87] op_sel:[1,1,0] op_sel_hi:[0,1,1] neg_lo:[0,1,0]\n"
        "v_cvt_pk_f16_f32 v122, v68, -v69\n"
        "v_cvt_pk_f16_f32 v123, v70, -v71\n"
        "ds_write_addtid_b32 v122 offset:288\n"
        "ds_write_addtid_b32 v123 offset:576\n"
        "v_pk_mul_f32 v[84:85], v[78:79], v[78:79] op_sel_hi:[1,0]\n"
        "v_pk_mul_f32 v[86:87], v[92:93], v[64:65] op_sel_hi:[1,0]\n"
        "v_pk_fma_f32 v[80:81], v[78:79], v[78:79], v[84:85] op_sel:[1,1,0] op_sel_hi:[0,1,1] neg_lo:[0,1,0]\n"
        "v_pk_fma_f32 v[96:97], v[92:93], v[64:65], v[86:87] op_sel:[1,1,0] op_sel_hi:[0,1,1] neg_lo:[0,1,0]\n"
        "v_cvt_pk_f16_f32 v124, v108, -v109\n"
        "v_cvt_pk_f16_f32 v125, v72, -v73\n"
        "ds_write_addtid_b32 v124 offset:864\n"
        "ds_write_addtid_b32 v125 offset:1152\n"
        "v_pk_mul_f16 v132, v122, v126 op_sel_hi:[1,0]\n"
        "v_pk_mul_f16 v133, v123, v126 op_sel_hi:[1,0]\n"
        "v_pk_fma_f16 v132, v122, v126, v132 op_sel:[1,1,0] op_sel_hi:[0,1,1] neg_lo:[0,1,0]\n"
        "v_pk_fma_f16 v133, v123, v126, v133 op_sel:[1,1,0] op_sel_hi:[0,1,1] neg_lo:[0,1,0]\n"
        "ds_write_addtid_b32 v132 offset:2592\n"
        "ds_write_addtid_b32 v133 offset:2880\n"
        "v_pk_mul_f32 v[84:85], v[92:93], v[66:67] op_sel_hi:[1,0]\n"
        "v_pk_mul_f32 v[86:87], v[94:95], v[66:67] op_sel_hi:[1,0]\n"
        "v_pk_fma_f32 v[100:101], v[92:93], v[66:67], v[84:85] op_sel:[1,1,0] op_sel_hi:[0,1,1] neg_lo:[0,1,0]\n"
        "v_pk_fma_f32 v[102:103], v[94:95], v[66:67], v[86:87] op_sel:[1,1,0] op_sel_hi:[0,1,1] neg_lo:[0,1,0]\n"
        "v_cvt_pk_f16_f32 v127, v110, -v111\n"
        "v_pk_mul_f16 v128, v124, v126 op_sel_hi:[1,0]\n"
        "v_pk_fma_f16 v128, v124, v126, v128 op_sel:[1,1,0] op_sel_hi:[0,1,1] neg_lo:[0,1,0]\n"
        "ds_write_addtid_b32 v127 offset:3456\n"
        "ds_write_addtid_b32 v128 offset:3168\n"
        "v_pk_mul_f16 v134, v122, v125 op_sel_hi:[1,0]\n"
        "v_pk_mul_f16 v135, v123, v125 op_sel_hi:[1,0]\n"
        "v_pk_fma_f16 v134, v122, v125, v134 op_sel:[1,1,0] op_sel_hi:[0,1,1] neg_lo:[0,1,0]\n"
        "v_pk_fma_f16 v135, v123, v125, v135 op_sel:[1,1,0] op_sel_hi:[0,1,1] neg_lo:[0,1,0]\n"
        "ds_write_addtid_b32 v134 offset:1440\n"
        "ds_write_addtid_b32 v135 offset:1728\n"
        "v_pk_mul_f32 v[84:85], v[96:97], v[66:67] op_sel_hi:[1,0]\n"
        "v_pk_mul_f32 v[86:87], v[76:77], v[78:79] op_sel_hi:[1,0]\n"
        "v_pk_fma_f32 v[104:105], v[96:97], v[66:67], v[84:85] op_sel:[1,1,0] op_sel_hi:[0,1,1] neg_lo:[0,1,0]\n"
        "v_pk_fma_f32 v[112:113], v[76:77], v[78:79], v[86:87] op_sel:[1,1,0] op_sel_hi:[0,1,1] neg_lo:[0,1,0]\n"
        "v_pk_mul_f16 v132, v124, v125 op_sel_hi:[1,0]\n"
        "v_pk_mul_f16 v133, v122, v127 op_sel_hi:[1,0]\n"
        "v_pk_fma_f16 v132, v124, v125, v132 op_sel:[1,1,0] op_sel_hi:[0,1,1] neg_lo:[0,1,0]\n"
        "v_pk_fma_f16 v133, v122, v127, v133 op_sel:[1,1,0] op_sel_hi:[0,1,1] neg_lo:[0,1,0]\n"
        "ds_write_addtid_b32 v132 offset:2016\n"
        "ds_write_addtid_b32 v133 offset:3744\n"
        "v_pk_mul_f16 v134, v123, v127 op_sel_hi:[1,0]\n"
        "v_pk_mul_f16 v135, v124, v127 op_sel_hi:[1,0]\n"
        "v_pk_fma_f16 v134, v123, v127, v134 op_sel:[1,1,0] op_sel_hi:[0,1,1] neg_lo:[0,1,0]\n"
        "v_pk_fma_f16 v135, v124, v127, v135 op_sel:[1,1,0] op_sel_hi:[0,1,1] neg_lo:[0,1,0]\n"
        "ds_write_addtid_b32 v134 offset:4032\n"
        "ds_write_addtid_b32 v135 offset:4320\n"
        "ds_read_b128 v[140:143], v10 offset:0\n"
        "ds_read_b128 v[144:147], v10 offset:64\n"
        "ds_read_b128 v[148:151], v10 offset:128\n"
        "ds_read_b128 v[152:155], v10 offset:192\n"
        "v_pk_mul_f32 v[84:85], v[80:81], v[80:81] op_sel_hi:[1,0]\n"
        "v_pk_mul_f32 v[86:87], v[106:107], v[80:81] op_sel_hi:[1,0]\n"
        "v_pk_fma_f32 v[82:83], v[80:81], v[80:81], v[84:85] op_sel:[1,1,0] op_sel_hi:[0,1,1] neg_lo:[0,1,0]\n"
        "v_pk_fma_f32 v[116:117], v[106:107], v[80:81], v[86:87] op_sel:[1,1,0] op_sel_hi:[0,1,1] neg_lo:[0,1,0]\n"
        "v_cvt_pk_f16_f32 v122, v92, v93\n"
        "v_cvt_pk_f16_f32 v123, v94, v95\n"
        "ds_write_addtid_b32 v122 offset:0\n"
        "ds_write_addtid_b32 v123 offset:288\n"
        "v_cvt_pk_f16_f32 v136, v76, v77\n"
        "v_pk_mul_f32 v[84:85], v[98:99], v[80:81] op_sel_hi:[1,0]\n"
        "v_pk_fma_f32 v[114:115], v[98:99], v[80:81], v[84:85] op_sel:[1,1,0] op_sel_hi:[0,1,1] neg_lo:[0,1,0]\n"
        "v_cvt_pk_f16_f32 v124, v96, v97\n"
        "v_cvt_pk_f16_f32 v125, v98, v99\n"
        "ds_write_addtid_b32 v124 offset:576\n"
        "ds_write_addtid_b32 v125 offset:864\n"
        "v_pk_mul_f32 v[84:85], v[106:107], v[82:83] op_sel_hi:[1,0]\n"
        "v_pk_mul_f32 v[86:87], v[116:117], v[82:83] op_sel_hi:[1,0]\n"
        "v_pk_fma_f32 v[118:119], v[106:107], v[82:83], v[84:85] op_sel:[1,1,0] op_sel_hi:[0,1,1] neg_lo:[0,1,0]\n"
        "v_pk_fma_f32 v[120:121], v[116:117], v[82:83], v[86:87] op_sel:[1,1,0] op_sel_hi:[0,1,1] neg_lo:[0,1,0]\n"
        "v_cvt_pk_f16_f32 v126, v100, v101\n"
        "v_cvt_pk_f16_f32 v127, v102, v103\n"
        "ds_write_addtid_b32 v126 offset:1152\n"
        "ds_write_addtid_b32 v127 offset:1440\n"
        "v_pk_mul_f16 v132, v122, v136 op_sel_hi:[1,0]\n"
        "v_pk_mul_f16 v133, v123, v136 op_sel_hi:[1,0]\n"
        "v_pk_fma_f16 v132, v122, v136, v132 op_sel:[1,1,0] op_sel_hi:[0,1,1] neg_lo:[0,1,0]\n"
        "v_pk_fma_f16 v133, v123, v136, v133 op_sel:[1,1,0] op_sel_hi:[0,1,1] neg_lo:[0,1,0]\n"
        "ds_write_addtid_b32 v132 offset:2304\n"
        "ds_write_addtid_b32 v133 offset:2592\n"
        "v_cvt_pk_f16_f32 v137, v78, v79\n"
        "v_cvt_pk_f16_f32 v138, v112, v113\n"
        "v_cvt_pk_f16_f32 v128, v104, v105\n"
        "v_cvt_pk_f16_f32 v129, v106, v107\n"
        "ds_write_addtid_b32 v128 offset:1728\n"
        "ds_write_addtid_b32 v129 offset:2016\n"
        "v_pk_mul_f16 v134, v124, v136 op_sel_hi:[1,0]\n"
        "v_pk_mul_f16 v135, v125, v136 op_sel_hi:[1,0]\n"
        "v_pk_fma_f16 v134, v124, v136, v134 op_sel:[1,1,0] op_sel_hi:[0,1,1] neg_lo:[0,1,0]\n"
        "v_pk_fma_f16 v135, v125, v136, v135 op_sel:[1,1,0] op_sel_hi:[0,1,1] neg_lo:[0,1,0]\n"
        "ds_write_addtid_b32 v134 offset:2880\n"
        "ds_write_addtid_b32 v135 offset:3168\n"
        "v_pk_mul_f16 v132, v126, v136 op_sel_hi:[1,0]\n"
        "v_pk_mul_f16 v133, v127, v136 op_sel_hi:[1,0]\n"
        "v_pk_fma_f16 v132, v126, v136, v132 op_sel:[1,1,0] op_sel_hi:[0,1,1] neg_lo:[0,1,0]\n"
        "v_pk_fma_f16 v133, v127, v136, v133 op_sel:[1,1,0] op_sel_hi:[0,1,1] neg_lo:[0,1,0]\n"
        "ds_write_addtid_b32 v132 offset:3456\n"
        "ds_write_addtid_b32 v133 offset:3744\n"
        "v_pk_mul_f16 v134, v128, v136 op_sel_hi:[1,0]\n"
        "v_pk_mul_f16 v135, v129, v136 op_sel_hi:[1,0]\n"
        "v_pk_fma_f16 v134, v128, v136, v134 op_sel:[1,1,0] op_sel_hi:[0,1,1] neg_lo:[0,1,0]\n"
        "v_pk_fma_f16 v135, v129, v136, v135 op_sel:[1,1,0] op_sel_hi:[0,1,1] neg_lo:[0,1,0]\n"
        "ds_write_addtid_b32 v134 offset:4032\n"
        "ds_write_addtid_b32 v135 offset:4320\n"
        "ds_read_b128 v[156:159], v10 offset:0\n"
        "ds_read_b128 v[160:163], v10 offset:64\n"
        "ds_read_b128 v[164:167], v10 offset:128\n"
        "ds_read_b128 v[168:171], v10 offset:192\n"
        "v_pk_mul_f16 v132, v123, v137 op_sel_hi:[1,0]\n"
        "v_pk_mul_f16 v133, v125, v137 op_sel_hi:[1,0]\n"
        "v_pk_fma_f16 v132, v123, v137, v132 op_sel:[1,1,0] op_sel_hi:[0,1,1] neg_lo:[0,1,0]\n"
        "v_pk_fma_f16 v133, v125, v137, v133 op_sel:[1,1,0] op_sel_hi:[0,1,1] neg_lo:[0,1,0]\n"
        "ds_write_addtid_b32 v132 offset:0\n"
        "ds_write_addtid_b32 v133 offset:288\n"
        "v_pk_mul_f16 v134, v127, v137 op_sel_hi:[1,0]\n"
        "v_pk_mul_f16 v135, v129, v137 op_sel_hi:[1,0]\n"
        "v_pk_fma_f16 v134, v127, v137, v134 op_sel:[1,1,0] op_sel_hi:[0,1,1] neg_lo:[0,1,0]\n"
        "v_pk_fma_f16 v135, v129, v137, v135 op_sel:[1,1,0] op_sel_hi:[0,1,1] neg_lo:[0,1,0]\n"
        "ds_write_addtid_b32 v134 offset:576\n"
        "ds_write_addtid_b32 v135 offset:864\n"
        "v_cvt_pk_f16_f32 v130, v114, v115\n"
        "v_cvt_pk_f16_f32 v131, v116, v117\n"
        "ds_write_addtid_b32 v130 offset:2304\n"
        "ds_write_addtid_b32 v131 offset:2592\n"
        "s_waitcnt lgkmcnt(9)\n"
        "v_mfma_f32_16x16x32_f16 v[186:189], v[156:159], v[140:143], 0\n"
        "v_pk_mul_f16 v132, v123, v138 op_sel_hi:[1,0]\n"
        "v_pk_mul_f16 v133, v125, v138 op_sel_hi:[1,0]\n"
        "v_pk_fma_f16 v132, v123, v138, v132 op_sel:[1,1,0] op_sel_hi:[0,1,1] neg_lo:[0,1,0]\n"
        "v_pk_fma_f16 v133, v125, v138, v133 op_sel:[1,1,0] op_sel_hi:[0,1,1] neg_lo:[0,1,0]\n"
        "ds_write_addtid_b32 v132 offset:1152\n"
        "ds_write_addtid_b32 v133 offset:1440\n"
        "s_waitcnt lgkmcnt(10)\n"
        "v_mfma_f32_16x16x32_f16 v[186:189], v[160:163], v[144:147], v[186:189]\n"
        "v_pk_mul_f16 v134, v127, v138 op_sel_hi:[1,0]\n"
        "v_pk_mul_f16 v135, v129, v138 op_sel_hi:[1,0]\n"
        "v_pk_fma_f16 v134, v127, v138, v134 op_sel:[1,1,0] op_sel_hi:[0,1,1] neg_lo:[0,1,0]\n"
        "v_pk_fma_f16 v135, v129, v138, v135 op_sel:[1,1,0] op_sel_hi:[0,1,1] neg_lo:[0,1,0]\n"
        "ds_write_addtid_b32 v134 offset:1728\n"
        "ds_write_addtid_b32 v135 offset:2016\n"
        "s_waitcnt lgkmcnt(11)\n"
        "v_mfma_f32_16x16x32_f16 v[186:189], v[164:167], v[148:151], v[186:189]\n"
        "v_pk_mul_f16 v132, v130, v136 op_sel_hi:[1,0]\n"
        "v_pk_mul_f16 v133, v131, v136 op_sel_hi:[1,0]\n"
        "v_pk_fma_f16 v132, v130, v136, v132 op_sel:[1,1,0] op_sel_hi:[0,1,1] neg_lo:[0,1,0]\n"
        "v_pk_fma_f16 v133, v131, v136, v133 op_sel:[1,1,0] op_sel_hi:[0,1,1] neg_lo:[0,1,0]\n"
        "ds_write_addtid_b32 v132 offset:2880\n"
        "ds_write_addtid_b32 v133 offset:3168\n"
        "s_waitcnt lgkmcnt(12)\n"
        "v_mfma_f32_16x16x32_f16 v[186:189], v[168:171], v[152:155], v[186:189]\n"
        "v_pk_mul_f16 v134, v130, v137 op_sel_hi:[1,0]\n"
        "v_pk_mul_f16 v135, v131, v137 op_sel_hi:[1,0]\n"
        "v_pk_fma_f16 v134, v130, v137, v134 op_sel:[1,1,0] op_sel_hi:[0,1,1] neg_lo:[0,1,0]\n"
        "v_pk_fma_f16 v135, v131, v137, v135 op_sel:[1,1,0] op_sel_hi:[0,1,1] neg_lo:[0,1,0]\n"
        "ds_write_addtid_b32 v134 offset:3456\n"
        "ds_write_addtid_b32 v135 offset:3744\n"
        "v_pk_mul_f16 v132, v130, v138 op_sel_hi:[1,0]\n"
        "v_pk_mul_f16 v133, v131, v138 op_sel_hi:[1,0]\n"
        "v_pk_fma_f16 v132, v130, v138, v132 op_sel:[1,1,0] op_sel_hi:[0,1,1] neg_lo:[0,1,0]\n"
        "v_pk_fma_f16 v133, v131, v138, v133 op_sel:[1,1,0] op_sel_hi:[0,1,1] neg_lo:[0,1,0]\n"
        "ds_write_addtid_b32 v132 offset:4032\n"
        "ds_write_addtid_b32 v133 offset:4320\n"
        "ds_read_b128 v[62:65], v10 offset:0\n"
        "ds_read_b128 v[66:69], v10 offset:64\n"
        "ds_read_b128 v[70:73], v10 offset:128\n"
        "ds_read_b128 v[74:77], v10 offset:192\n"
        "v_cvt_pk_f16_f32 v126, v56, v57\n"
        "v_cvt_pk_f16_f32 v122, v118, v119\n"
        "v_cvt_pk_f16_f32 v124, v120, v121\n"
        "ds_write_addtid_b32 v122 offset:0\n"
        "ds_write_addtid_b32 v124 offset:1152\n"
        "ds_write_addtid_b32 v126 offset:2304\n"
        "v_pk_mul_f16 v134, v122, v136 op_sel_hi:[1,0]\n"
        "v_pk_mul_f16 v135, v124, v136 op_sel_hi:[1,0]\n"
        "v_pk_fma_f16 v134, v122, v136, v134 op_sel:[1,1,0] op_sel_hi:[0,1,1] neg_lo:[0,1,0]\n"
        "v_pk_fma_f16 v135, v124, v136, v135 op_sel:[1,1,0] op_sel_hi:[0,1,1] neg_lo:[0,1,0]\n"
        "ds_write_addtid_b32 v134 offset:288\n"
        "ds_write_addtid_b32 v135 offset:1440\n"
        "v_pk_mul_f16 v132, v122, v137 op_sel_hi:[1,0]\n"
        "v_pk_mul_f16 v133, v124, v137 op_sel_hi:[1,0]\n"
        "v_pk_fma_f16 v132, v122, v137, v132 op_sel:[1,1,0] op_sel_hi:[0,1,1] neg_lo:[0,1,0]\n"
        "v_pk_fma_f16 v133, v124, v137, v133 op_sel:[1,1,0] op_sel_hi:[0,1,1] neg_lo:[0,1,0]\n"
        "ds_write_addtid_b32 v132 offset:576\n"
        "ds_write_addtid_b32 v133 offset:1728\n"
        "v_pk_mul_f16 v134, v122, v138 op_sel_hi:[1,0]\n"
        "v_pk_mul_f16 v135, v124, v138 op_sel_hi:[1,0]\n"
        "v_pk_fma_f16 v134, v122, v138, v134 op_sel:[1,1,0] op_sel_hi:[0,1,1] neg_lo:[0,1,0]\n"
        "v_pk_fma_f16 v135, v124, v138, v135 op_sel:[1,1,0] op_sel_hi:[0,1,1] neg_lo:[0,1,0]\n"
        "ds_write_addtid_b32 v134 offset:864\n"
        "ds_write_addtid_b32 v135 offset:2016\n"
        "ds_read_b128 v[156:159], v10 offset:0\n"
        "ds_read_b128 v[160:163], v10 offset:64\n"
        "ds_read_b128 v[164:167], v10 offset:128\n"
        "ds_read_b128 v[168:171], v10 offset:192\n"
        "s_waitcnt lgkmcnt(14)\n"
        "v_mfma_f32_16x16x32_f16 v[190:193], v[62:65], v[140:143], 0\n"
        "v_mfma_f32_16x16x32_f16 v[190:193], v[66:69], v[144:147], v[190:193]\n"
        "v_mfma_f32_16x16x32_f16 v[190:193], v[70:73], v[148:151], v[190:193]\n"
        "s_waitcnt lgkmcnt(13)\n"
        "v_mfma_f32_16x16x32_f16 v[190:193], v[74:77], v[152:155], v[190:193]\n"
        "s_waitcnt lgkmcnt(3)\n"
        "v_mfma_f32_16x16x32_f16 v[194:197], v[156:159], v[140:143], 0\n"
        "s_waitcnt lgkmcnt(2)\n"
        "v_mfma_f32_16x16x32_f16 v[194:197], v[160:163], v[144:147], v[194:197]\n"
        "s_waitcnt lgkmcnt(1)\n"
        "v_mfma_f32_16x16x32_f16 v[194:197], v[164:167], v[148:151], v[194:197]\n"
        "s_waitcnt lgkmcnt(0)\n"
        "v_mfma_f32_16x16x32_f16 v[194:197], v[168:171], v[152:155], v[194:197]\n"
        "s_nop 0\n"
        "s_barrier\n"
        "ds_read_b128 v[198:201], v11 offset:0\n"
        "ds_read_b128 v[202:205], v11 offset:1024\n"
        "ds_read_b128 v[206:209], v11 offset:2048\n"
        "s_waitcnt vmcnt(0)\n"
        "s_waitcnt lgkmcnt(2)\n"
        "v_pk_add_f32 v[186:187], v[186:187], v[198:199]\n"
        "v_pk_add_f32 v[188:189], v[188:189], v[200:201]\n"
        "v_cvt_pk_f16_f32 v210, v189, v188\n"
        "v_cvt_pk_f16_f32 v211, v187, v186\n"
        "v_cvt_pk_f16_f32 v212, v188, v187\n"
        "ds_write_b64 v12, v[210:211] offset:2296\n"
        "s_waitcnt lgkmcnt(2)\n"
        "v_pk_add_f32 v[190:191], v[190:191], v[202:203]\n"
        "v_pk_add_f32 v[192:193], v[192:193], v[204:205]\n"
        "ds_write_b16 v12, v210 offset:1078\n"
        "s_waitcnt lgkmcnt(2)\n"
        "v_pk_add_f32 v[194:195], v[194:195], v[206:207]\n"
        "v_pk_add_f32 v[196:197], v[196:197], v[208:209]\n"
        "ds_write_b32 v12, v212 offset:1080\n"
        "v_cvt_pk_f16_f32 v214, v193, v193\n"
        "v_cvt_pk_f16_f32 v219, v194, v195\n"
        "ds_write_b16_d16_hi v12, v211 offset:1084\n"
        "v_mov_b32_e64 v221, v210\n"
        "v_cvt_pk_f16_f32 v216, v193, v192\n"
        "v_cvt_pk_f16_f32 v217, v191, v191\n"
        "v_cvt_pk_f16_f32 v218, v191, v190\n"
        "v_cvt_pk_f16_f32 v215, v192, v191\n"
        "v_cvt_pk_f16_f32 v220, v196, v197\n"
        "v_readlane_b32 s45, v214, 63\n"
        "s_mov_b32 exec_hi, 0\n"
        "s_brev_b32 exec_lo, 1\n"
        "v_mov_b32 v221, v214\n"
        "s_mov_b32 exec_hi, 1\n"
        "s_mov_b32 exec_lo, 0\n"
        "v_mov_b32_e64 v221, v219\n"
        "s_mov_b32 exec_hi, 0x80000001\n"
        "s_brev_b32 exec_lo, 1\n"
        "ds_write_b16 v15, v221\n"
        "ds_write_b16 v15, v221 offset:1218\n"
        "s_mov_b32 exec_hi, 0\n"
        "s_mov_b32 exec_lo, -1\n"
        "ds_write_b32 v13, v214 offset:1068\n"
        "ds_write_b64 v13, v[216:217] offset:1072\n"
        "ds_write_b32 v13, v218 offset:1080\n"
        "ds_write_b16 v13, v214 offset:2286\n"
        "ds_write_b64 v13, v[214:215] offset:2288\n"
        "ds_write_b32 v13, v217 offset:2296\n"
        "ds_write_b16_d16_hi v13, v218 offset:2300\n"
        "ds_write_b16 v14, v219 offset:96\n"
        "ds_write_b16_d16_hi v14, v219 offset:64\n"
        "ds_write_b16 v14, v220 offset:32\n"
        "ds_write_b16_d16_hi v14, v220\n"
        "s_not_b64 exec, exec\n"
        "ds_write2_b32 v14, v216, v218 offset1:16\n"
        "s_mov_b64 exec, -1\n"
        "ds_read2_b32 v[46:47], v16 offset0:240 offset1:241\n"
        "ds_read2_b32 v[48:49], v16 offset0:242 offset1:243\n"
        "ds_read2_b32 v[50:51], v16 offset0:224 offset1:225\n"
        "ds_read2_b32 v[52:53], v16 offset0:226 offset1:227\n"
        "ds_read2_b32 v[54:55], v16 offset0:208 offset1:209\n"
        "ds_read2_b32 v[56:57], v16 offset0:210 offset1:211\n"
        "ds_read2_b32 v[58:59], v16 offset0:192 offset1:193\n"
        "ds_read2_b32 v[60:61], v16 offset0:194 offset1:195\n"
        "ds_read2_b32 v[62:63], v16 offset0:176 offset1:177\n"
        "ds_read2_b32 v[64:65], v16 offset0:178 offset1:179\n"
        "ds_read2_b32 v[66:67], v16 offset0:160 offset1:161\n"
        "ds_read2_b32 v[68:69], v16 offset0:162 offset1:163\n"
        "ds_read2_b32 v[70:71], v16 offset0:144 offset1:145\n"
        "ds_read2_b32 v[72:73], v16 offset0:146 offset1:147\n"
        "ds_read2_b32 v[74:75], v16 offset0:128 offset1:129\n"
        "ds_read2_b32 v[76:77], v16 offset0:130 offset1:131\n"
        "ds_read2_b32 v[78:79], v16 offset0:112 offset1:113\n"
        "ds_read2_b32 v[80:81], v16 offset0:114 offset1:115\n"
        "ds_read2_b32 v[82:83], v16 offset0:96 offset1:97\n"
        "ds_read2_b32 v[84:85], v16 offset0:98 offset1:99\n"
        "ds_read2_b32 v[86:87], v16 offset0:80 offset1:81\n"
        "ds_read2_b32 v[88:89], v16 offset0:82 offset1:83\n"
        "ds_read2_b32 v[90:91], v16 offset0:64 offset1:65\n"
        "ds_read2_b32 v[92:93], v16 offset0:66 offset1:67\n"
        "ds_read2_b32 v[94:95], v16 offset0:48 offset1:49\n"
        "ds_read2_b32 v[96:97], v16 offset0:50 offset1:51\n"
        "ds_read2_b32 v[98:99], v16 offset0:32 offset1:33\n"
        "ds_read2_b32 v[100:101], v16 offset0:34 offset1:35\n"
        "ds_read2_b32 v[102:103], v16 offset0:16 offset1:17\n"
        "ds_read2_b32 v[104:105], v16 offset0:18 offset1:19\n"
        "ds_read2_b32 v[106:107], v16 offset0:0 offset1:1\n"
        "ds_read2_b32 v[108:109], v16 offset0:2 offset1:3\n"
        "ds_read2_b32 v[110:111], v17 offset0:0 offset1:1\n"
        "ds_read2_b32 v[112:113], v17 offset0:2 offset1:3\n"
        "v_pk_mul_f16 v42, v42, s45\n"
        "v_pk_mul_f16 v43, v43, s45\n"
        "v_pk_mul_f16 v44, v44, s45\n"
        "v_pk_mul_f16 v45, v45, s45\n"
        "s_lshl_b32 s38, s33, 5\n"
        "s_add_u32 s38, s38, 131072\n"
        "v_lshrrev_b32 v4, 1, v1\n"
        "v_and_b32 v5, 1, v1\n"
        "v_lshl_add_u32 v6, v5, 4, s38\n"
        "v_lshl_add_u32 v24, v4, 12, v6\n"
        "v_min_u32 v4, 10, v4\n"
        "v_add_u32_e64 v4, 32, v4\n"
        "v_lshl_add_u32 v25, v4, 12, v6\n"
        "s_lshr_b32 s38, s33, 2\n"
        "s_lshl_b32 s38, s38, 7\n"
        "s_add_u32 s38, s38, 65536\n"
        "v_and_b32 v9, 16, v1\n"
        "v_add_u32 v9, s38, v9\n"
        "s_add_u32 s46, s40, 1\n"
        "s_and_b32 s56, s46, 0xff\n"
        "s_bfe_u32 s57, s46, 0x80008\n"
        "s_lshl_b32 s58, s46, 16\n"
        "s_mov_b32 s47, 32768\n"
        "s_cmp_eq_u32 s30, 0\n"
        "s_barrier\n"
        "ds_read_b128 v[186:189], v20 offset:448\n"
        "ds_read_b128 v[190:193], v20 offset:384\n"
        "ds_read_b128 v[194:197], v20 offset:320\n"
        "ds_read_b128 v[198:201], v20 offset:256\n"
        "ds_read_b128 v[202:205], v20 offset:192\n"
        "ds_read_b128 v[206:209], v20 offset:128\n"
        "ds_read_b128 v[210:213], v20 offset:64\n"
        "ds_read_b128 v[214:217], v20 offset:0\n"
        "s_waitcnt lgkmcnt(7)\n"
        "v_mfma_f32_16x16x32_f16 v[154:157], v[46:49], v[186:189], 0\n"
        "ds_read_b64 v[118:119], v18 offset:256\n"
        "s_waitcnt lgkmcnt(7)\n"
        "v_mfma_f32_16x16x32_f16 v[154:157], v[50:53], v[190:193], v[154:157]\n"
        "s_waitcnt lgkmcnt(6)\n"
        "v_mfma_f32_16x16x32_f16 v[154:157], v[54:57], v[194:197], v[154:157]\n"
        "ds_read_b64 v[120:121], v18 offset:264\n"
        "s_waitcnt lgkmcnt(6)\n"
        "v_mfma_f32_16x16x32_f16 v[154:157], v[58:61], v[198:201], v[154:157]\n"
        "ds_read_b64 v[126:127], v18 offset:128\n"
        "s_waitcnt lgkmcnt(6)\n"
        "v_mfma_f32_16x16x32_f16 v[154:157], v[62:65], v[202:205], v[154:157]\n"
        "ds_read_b64 v[128:129], v18 offset:136\n"
        "s_waitcnt lgkmcnt(6)\n"
        "v_mfma_f32_16x16x32_f16 v[154:157], v[66:69], v[206:209], v[154:157]\n"
        "ds_read_b64 v[134:135], v18 offset:0\n"
        "s_waitcnt lgkmcnt(6)\n"
        "v_mfma_f32_16x16x32_f16 v[154:157], v[70:73], v[210:213], v[154:157]\n"
        "ds_read_b64 v[136:137], v18 offset:8\n"
        "s_waitcnt lgkmcnt(6)\n"
        "v_mfma_f32_16x16x32_f16 v[154:157], v[74:77], v[214:217], v[154:157]\n"
        "ds_read_b64 v[114:115], v18 offset:320\n"
        "v_mfma_f32_16x16x32_f16 v[158:161], v[78:81], v[186:189], 0\n"
        "ds_read_b64 v[116:117], v18 offset:328\n"
        "v_mfma_f32_16x16x32_f16 v[162:165], v[110:113], v[186:189], 0\n"
        "ds_read_b64 v[122:123], v18 offset:192\n"
        "v_mfma_f32_16x16x32_f16 v[170:173], v[42:45], v[186:189], 0\n"
        "ds_read_b128 v[186:189], v21 offset:0\n"
        "v_mfma_f32_16x16x32_f16 v[158:161], v[82:85], v[190:193], v[158:161]\n"
        "ds_read_b128 v[190:193], v21 offset:256\n"
        "ds_write_b128 v22, v[154:157] offset:0\n"
        "v_mfma_f32_16x16x32_f16 v[158:161], v[86:89], v[194:197], v[158:161]\n"
        "v_mfma_f32_16x16x32_f16 v[158:161], v[90:93], v[198:201], v[158:161]\n"
        "ds_read_b128 v[194:197], v20 offset:960\n"
        "ds_add_u32 v23, v254 offset:0\n"
        "v_mfma_f32_16x16x32_f16 v[158:161], v[94:97], v[202:205], v[158:161]\n"
        "ds_read_b128 v[198:201], v20 offset:896\n"
        "v_mfma_f32_16x16x32_f16 v[158:161], v[98:101], v[206:209], v[158:161]\n"
        "v_mfma_f32_16x16x32_f16 v[158:161], v[102:105], v[210:213], v[158:161]\n"
        "ds_read_b128 v[202:205], v20 offset:832\n"
        "v_mfma_f32_16x16x32_f16 v[158:161], v[106:109], v[214:217], v[158:161]\n"
        "s_waitcnt lgkmcnt(6)\n"
        "v_mfma_f32_16x16x32_f16 v[218:221], v[186:189], v[26:29], 0\n"
        "ds_read_b128 v[206:209], v20 offset:768\n"
        "v_mfma_f32_16x16x32_f16 v[222:225], v[186:189], v[30:33], 0\n"
        "ds_read_b128 v[210:213], v20 offset:704\n"
        "s_waitcnt lgkmcnt(6)\n"
        "v_mfma_f32_16x16x32_f16 v[226:229], v[190:193], v[26:29], 0\n"
        "ds_read_b128 v[214:217], v20 offset:640\n"
        "v_mfma_f32_16x16x32_f16 v[230:233], v[190:193], v[30:33], 0\n"
        "ds_read_b128 v[186:189], v20 offset:576\n"
        "s_waitcnt lgkmcnt(6)\n"
        "v_mfma_f32_16x16x32_f16 v[158:161], v[46:49], v[194:197], v[158:161]\n"
        "v_mfma_f32_16x16x32_f16 v[162:165], v[78:81], v[194:197], v[162:165]\n"
        "ds_read_b128 v[190:193], v20 offset:512\n"
        "v_mfma_f32_16x16x32_f16 v[166:169], v[110:113], v[194:197], 0\n"
        "v_mfma_f32_16x16x32_f16 v[174:177], v[42:45], v[194:197], 0\n"
        "ds_read_b64 v[124:125], v18 offset:200\n"
        "s_waitcnt lgkmcnt(6)\n"
        "v_mfma_f32_16x16x32_f16 v[158:161], v[50:53], v[198:201], v[158:161]\n"
        "v_cvt_pk_f16_f32 v242, v218, v222\n"
        "v_cvt_pk_f16_f32 v243, v219, v223\n"
        "v_mfma_f32_16x16x32_f16 v[162:165], v[82:85], v[198:201], v[162:165]\n"
        "ds_read_b64 v[130:131], v18 offset:64\n"
        "v_mfma_f32_16x16x32_f16 v[158:161], v[54:57], v[202:205], v[158:161]\n"
        "v_cvt_pk_f16_f32 v244, v220, v224\n"
        "v_cvt_pk_f16_f32 v245, v221, v225\n"
        "v_mfma_f32_16x16x32_f16 v[162:165], v[86:89], v[202:205], v[162:165]\n"
        "ds_read_b64 v[132:133], v18 offset:72\n"
        "s_waitcnt lgkmcnt(4)\n"
        "v_mfma_f32_16x16x32_f16 v[158:161], v[58:61], v[206:209], v[158:161]\n"
        "v_cvt_pk_f16_f32 v246, v226, v230\n"
        "v_cvt_pk_f16_f32 v247, v227, v231\n"
        "v_mfma_f32_16x16x32_f16 v[162:165], v[90:93], v[206:209], v[162:165]\n"
        "ds_read_b128 v[194:197], v21 offset:512\n"
        "v_mfma_f32_16x16x32_f16 v[158:161], v[62:65], v[210:213], v[158:161]\n"
        "ds_read_b128 v[198:201], v21 offset:768\n"
        "v_mfma_f32_16x16x32_f16 v[162:165], v[94:97], v[210:213], v[162:165]\n"
        "v_cvt_pk_f16_f32 v248, v228, v232\n"
        "v_cvt_pk_f16_f32 v249, v229, v233\n"
        "v_mfma_f32_16x16x32_f16 v[158:161], v[66:69], v[214:217], v[158:161]\n"
        "ds_read_b128 v[202:205], v20 offset:1472\n"
        "v_mfma_f32_16x16x32_f16 v[158:161], v[70:73], v[186:189], v[158:161]\n"
        "s_waitcnt lgkmcnt(4)\n"
        "v_mfma_f32_16x16x32_f16 v[158:161], v[74:77], v[190:193], v[158:161]\n"
        "ds_read_b128 v[206:209], v20 offset:1408\n"
        "v_mfma_f32_16x16x32_f16 v[162:165], v[98:101], v[214:217], v[162:165]\n"
        "ds_read_b128 v[210:213], v20 offset:1344\n"
        "v_mfma_f32_16x16x32_f16 v[162:165], v[102:105], v[186:189], v[162:165]\n"
        "ds_read_b128 v[214:217], v20 offset:1280\n"
        "v_mfma_f32_16x16x32_f16 v[162:165], v[106:109], v[190:193], v[162:165]\n"
        "ds_read_b128 v[186:189], v20 offset:1216\n"
        "v_mfma_f32_16x16x32_f16 v[162:165], v[242:245], v[118:121], v[162:165]\n"
        "ds_write_b128 v22, v[158:161] offset:1280\n"
        "v_mfma_f32_16x16x32_f16 v[166:169], v[242:245], v[126:129], v[166:169]\n"
        "v_mfma_f32_16x16x32_f16 v[170:173], v[242:245], v[134:137], v[170:173]\n"
        "ds_add_u32 v23, v254 offset:256\n"
        "v_mfma_f32_16x16x32_f16 v[162:165], v[246:249], v[114:117], v[162:165]\n"
        "v_mfma_f32_16x16x32_f16 v[166:169], v[246:249], v[122:125], v[166:169]\n"
        "ds_read_b128 v[190:193], v20 offset:1152\n"
        "s_waitcnt lgkmcnt(6)\n"
        "v_mfma_f32_16x16x32_f16 v[170:173], v[246:249], v[130:133], v[170:173]\n"
        "v_mfma_f32_16x16x32_f16 v[218:221], v[194:197], v[26:29], 0\n"
        "ds_read2_b32 v[138:139], v19 offset0:48 offset1:49\n"
        "v_mfma_f32_16x16x32_f16 v[222:225], v[194:197], v[30:33], 0\n"
        "v_mfma_f32_16x16x32_f16 v[226:229], v[198:201], v[26:29], 0\n"
        "ds_read_b128 v[194:197], v20 offset:1088\n"
        "v_mfma_f32_16x16x32_f16 v[230:233], v[198:201], v[30:33], 0\n"
        "v_mfma_f32_16x16x32_f16 v[162:165], v[46:49], v[202:205], v[162:165]\n"
        "ds_read_b128 v[198:201], v20 offset:1024\n"
        "v_mfma_f32_16x16x32_f16 v[166:169], v[78:81], v[202:205], v[166:169]\n"
        "v_mfma_f32_16x16x32_f16 v[170:173], v[110:113], v[202:205], v[170:173]\n"
        "ds_read2_b32 v[140:141], v19 offset0:50 offset1:51\n"
        "v_mfma_f32_16x16x32_f16 v[178:181], v[42:45], v[202:205], 0\n"
        "v_cvt_pk_f16_f32 v242, v218, v222\n"
        "v_cvt_pk_f16_f32 v243, v219, v223\n"
        "v_mfma_f32_16x16x32_f16 v[162:165], v[50:53], v[206:209], v[162:165]\n"
        "ds_read_b128 v[202:205], v21 offset:1024\n"
        "v_mfma_f32_16x16x32_f16 v[166:169], v[82:85], v[206:209], v[166:169]\n"
        "v_cvt_pk_f16_f32 v244, v220, v224\n"
        "v_cvt_pk_f16_f32 v245, v221, v225\n"
        "s_waitcnt lgkmcnt(4)\n"
        "v_mfma_f32_16x16x32_f16 v[162:165], v[54:57], v[210:213], v[162:165]\n"
        "ds_read_b128 v[206:209], v21 offset:1280\n"
        "v_mfma_f32_16x16x32_f16 v[166:169], v[86:89], v[210:213], v[166:169]\n"
        "v_cvt_pk_f16_f32 v246, v226, v230\n"
        "v_cvt_pk_f16_f32 v247, v227, v231\n"
        "v_mfma_f32_16x16x32_f16 v[162:165], v[58:61], v[214:217], v[162:165]\n"
        "ds_read_b128 v[210:213], v20 offset:432\n"
        "v_mfma_f32_16x16x32_f16 v[166:169], v[90:93], v[214:217], v[166:169]\n"
        "v_cvt_pk_f16_f32 v248, v228, v232\n"
        "v_cvt_pk_f16_f32 v249, v229, v233\n"
        "v_mfma_f32_16x16x32_f16 v[162:165], v[62:65], v[186:189], v[162:165]\n"
        "ds_read_b128 v[214:217], v20 offset:448\n"
        "v_mfma_f32_16x16x32_f16 v[166:169], v[94:97], v[186:189], v[166:169]\n"
        "v_mfma_f32_16x16x32_f16 v[162:165], v[66:69], v[190:193], v[162:165]\n"
        "ds_read_b128 v[186:189], v20 offset:1984\n"
        "s_waitcnt lgkmcnt(4)\n"
        "v_mfma_f32_16x16x32_f16 v[162:165], v[70:73], v[194:197], v[162:165]\n"
        "v_mfma_f32_16x16x32_f16 v[162:165], v[74:77], v[198:201], v[162:165]\n"
        "ds_read2_b32 v[142:143], v19 offset0:32 offset1:33\n"
        "v_mfma_f32_16x16x32_f16 v[166:169], v[98:101], v[190:193], v[166:169]\n"
        "ds_read_b128 v[190:193], v20 offset:1920\n"
        "v_mfma_f32_16x16x32_f16 v[166:169], v[102:105], v[194:197], v[166:169]\n"
        "ds_read_b128 v[194:197], v20 offset:1856\n"
        "v_mfma_f32_16x16x32_f16 v[166:169], v[106:109], v[198:201], v[166:169]\n"
        "ds_read_b128 v[198:201], v20 offset:1792\n"
        "v_mfma_f32_16x16x32_f16 v[166:169], v[242:245], v[118:121], v[166:169]\n"
        "ds_write_b128 v22, v[162:165] offset:2560\n"
        "v_mfma_f32_16x16x32_f16 v[170:173], v[242:245], v[126:129], v[170:173]\n"
        "v_mfma_f32_16x16x32_f16 v[174:177], v[242:245], v[134:137], v[174:177]\n"
        "ds_add_u32 v23, v254 offset:512\n"
        "v_mfma_f32_16x16x32_f16 v[166:169], v[246:249], v[114:117], v[166:169]\n"
        "v_mfma_f32_16x16x32_f16 v[170:173], v[246:249], v[122:125], v[170:173]\n"
        "ds_read2_b32 v[144:145], v19 offset0:34 offset1:35\n"
        "v_mfma_f32_16x16x32_f16 v[174:177], v[246:249], v[130:133], v[174:177]\n"
        "v_mfma_f32_16x16x32_f16 v[218:221], v[202:205], v[26:29], 0\n"
        "ds_read2_b32 v[146:147], v19 offset0:16 offset1:17\n"
        "v_mfma_f32_16x16x32_f16 v[222:225], v[202:205], v[30:33], 0\n"
        "s_waitcnt lgkmcnt(4)\n"
        "v_mfma_f32_16x16x32_f16 v[226:229], v[206:209], v[26:29], 0\n"
        "ds_read_b128 v[202:205], v20 offset:1728\n"
        "v_mfma_f32_16x16x32_f16 v[230:233], v[206:209], v[30:33], 0\n"
        "v_mfma_f32_16x16x32_f16 v[234:237], v[210:213], v[34:37], 0\n"
        "ds_read_b128 v[206:209], v20 offset:1664\n"
        "v_mfma_f32_16x16x32_f16 v[238:241], v[214:217], v[38:41], 0\n"
        "v_mfma_f32_16x16x32_f16 v[166:169], v[46:49], v[186:189], v[166:169]\n"
        "ds_read_b128 v[210:213], v20 offset:1600\n"
        "v_mfma_f32_16x16x32_f16 v[170:173], v[78:81], v[186:189], v[170:173]\n"
        "v_cvt_pk_f16_f32 v242, v218, v222\n"
        "v_cvt_pk_f16_f32 v243, v219, v223\n"
        "v_mfma_f32_16x16x32_f16 v[174:177], v[110:113], v[186:189], v[174:177]\n"
        "ds_read_b128 v[214:217], v20 offset:1536\n"
        "v_mfma_f32_16x16x32_f16 v[182:185], v[42:45], v[186:189], 0\n"
        "v_cvt_pk_f16_f32 v244, v220, v224\n"
        "v_cvt_pk_f16_f32 v245, v221, v225\n"
        "v_mfma_f32_16x16x32_f16 v[166:169], v[50:53], v[190:193], v[166:169]\n"
        "ds_read2_b32 v[148:149], v19 offset0:18 offset1:19\n"
        "v_mfma_f32_16x16x32_f16 v[170:173], v[82:85], v[190:193], v[170:173]\n"
        "v_cvt_pk_f16_f32 v246, v226, v230\n"
        "v_cvt_pk_f16_f32 v247, v227, v231\n"
        "v_mfma_f32_16x16x32_f16 v[166:169], v[54:57], v[194:197], v[166:169]\n"
        "ds_read2_b32 v[150:151], v19 offset0:0 offset1:1\n"
        "v_mfma_f32_16x16x32_f16 v[170:173], v[86:89], v[194:197], v[170:173]\n"
        "v_cvt_pk_f16_f32 v248, v228, v232\n"
        "v_cvt_pk_f16_f32 v249, v229, v233\n"
        "v_mfma_f32_16x16x32_f16 v[166:169], v[58:61], v[198:201], v[166:169]\n"
        "ds_read2_b32 v[152:153], v19 offset0:2 offset1:3\n"
        "v_mfma_f32_16x16x32_f16 v[170:173], v[90:93], v[198:201], v[170:173]\n"
        "v_cvt_pk_f16_f32 v250, v234, v238\n"
        "v_cvt_pk_f16_f32 v251, v235, v239\n"
        "s_waitcnt lgkmcnt(4)\n"
        "v_mfma_f32_16x16x32_f16 v[166:169], v[62:65], v[202:205], v[166:169]\n"
        "ds_read_b128 v[186:189], v21 offset:1536\n"
        "v_mfma_f32_16x16x32_f16 v[170:173], v[94:97], v[202:205], v[170:173]\n"
        "v_cvt_pk_f16_f32 v252, v236, v240\n"
        "v_cvt_pk_f16_f32 v253, v237, v241\n"
        "v_mfma_f32_16x16x32_f16 v[166:169], v[66:69], v[206:209], v[166:169]\n"
        "ds_read_b128 v[190:193], v21 offset:1792\n"
        "v_mfma_f32_16x16x32_f16 v[166:169], v[70:73], v[210:213], v[166:169]\n"
        "s_waitcnt lgkmcnt(4)\n"
        "v_mfma_f32_16x16x32_f16 v[166:169], v[74:77], v[214:217], v[166:169]\n"
        "ds_read_b128 v[194:197], v20 offset:944\n"
        "v_mfma_f32_16x16x32_f16 v[170:173], v[98:101], v[206:209], v[170:173]\n"
        "ds_read_b128 v[198:201], v20 offset:960\n"
        "v_mfma_f32_16x16x32_f16 v[170:173], v[102:105], v[210:213], v[170:173]\n"
        "ds_read_b128 v[202:205], v20 offset:2496\n"
        "v_mfma_f32_16x16x32_f16 v[170:173], v[106:109], v[214:217], v[170:173]\n"
        "ds_read_b128 v[206:209], v20 offset:2432\n"
        "v_mfma_f32_16x16x32_f16 v[170:173], v[242:245], v[118:121], v[170:173]\n"
        "ds_write_b128 v22, v[166:169] offset:3840\n"
        "v_mfma_f32_16x16x32_f16 v[174:177], v[242:245], v[126:129], v[174:177]\n"
        "v_mfma_f32_16x16x32_f16 v[178:181], v[242:245], v[134:137], v[178:181]\n"
        "ds_add_u32 v23, v254 offset:768\n"
        "v_mfma_f32_16x16x32_f16 v[170:173], v[246:249], v[114:117], v[170:173]\n"
        "v_mfma_f32_16x16x32_f16 v[174:177], v[246:249], v[122:125], v[174:177]\n"
        "ds_read_b128 v[210:213], v20 offset:2368\n"
        "v_mfma_f32_16x16x32_f16 v[178:181], v[246:249], v[130:133], v[178:181]\n"
        "v_mfma_f32_16x16x32_f16 v[170:173], v[250:253], v[138:141], v[170:173]\n"
        "ds_read_b128 v[214:217], v20 offset:2304\n"
        "v_mfma_f32_16x16x32_f16 v[174:177], v[250:253], v[142:145], v[174:177]\n"
        "v_mfma_f32_16x16x32_f16 v[178:181], v[250:253], v[146:149], v[178:181]\n"
        "s_waitcnt lgkmcnt(3)\n"
        "v_mfma_f32_16x16x32_f16 v[182:185], v[250:253], v[150:153], v[182:185]\n"
        "v_mfma_f32_16x16x32_f16 v[218:221], v[186:189], v[26:29], 0\n"
        "v_mfma_f32_16x16x32_f16 v[222:225], v[186:189], v[30:33], 0\n"
        "ds_read_b128 v[186:189], v20 offset:2240\n"
        "v_mfma_f32_16x16x32_f16 v[226:229], v[190:193], v[26:29], 0\n"
        "v_mfma_f32_16x16x32_f16 v[230:233], v[190:193], v[30:33], 0\n"
        "ds_read_b128 v[190:193], v20 offset:2176\n"
        "v_mfma_f32_16x16x32_f16 v[234:237], v[194:197], v[34:37], 0\n"
        "v_mfma_f32_16x16x32_f16 v[238:241], v[198:201], v[38:41], 0\n"
        "ds_read_b128 v[194:197], v20 offset:2112\n"
        "v_mfma_f32_16x16x32_f16 v[170:173], v[46:49], v[202:205], v[170:173]\n"
        "v_mfma_f32_16x16x32_f16 v[174:177], v[78:81], v[202:205], v[174:177]\n"
        "ds_read_b128 v[198:201], v20 offset:2048\n"
        "v_mfma_f32_16x16x32_f16 v[178:181], v[110:113], v[202:205], v[178:181]\n"
        "v_cvt_pk_f16_f32 v242, v218, v222\n"
        "v_cvt_pk_f16_f32 v243, v219, v223\n"
        "v_mfma_f32_16x16x32_f16 v[170:173], v[50:53], v[206:209], v[170:173]\n"
        "v_cvt_pk_f16_f32 v244, v220, v224\n"
        "v_cvt_pk_f16_f32 v245, v221, v225\n"
        "v_mfma_f32_16x16x32_f16 v[174:177], v[82:85], v[206:209], v[174:177]\n"
        "ds_read_b128 v[202:205], v21 offset:2048\n"
        "s_waitcnt lgkmcnt(4)\n"
        "v_mfma_f32_16x16x32_f16 v[170:173], v[54:57], v[210:213], v[170:173]\n"
        "v_cvt_pk_f16_f32 v246, v226, v230\n"
        "v_cvt_pk_f16_f32 v247, v227, v231\n"
        "v_mfma_f32_16x16x32_f16 v[174:177], v[86:89], v[210:213], v[174:177]\n"
        "v_cvt_pk_f16_f32 v248, v228, v232\n"
        "v_cvt_pk_f16_f32 v249, v229, v233\n"
        "v_mfma_f32_16x16x32_f16 v[170:173], v[58:61], v[214:217], v[170:173]\n"
        "ds_read_b128 v[206:209], v21 offset:2304\n"
        "v_mfma_f32_16x16x32_f16 v[174:177], v[90:93], v[214:217], v[174:177]\n"
        "v_cvt_pk_f16_f32 v250, v234, v238\n"
        "v_cvt_pk_f16_f32 v251, v235, v239\n"
        "v_mfma_f32_16x16x32_f16 v[170:173], v[62:65], v[186:189], v[170:173]\n"
        "ds_read_b128 v[210:213], v20 offset:1456\n"
        "v_mfma_f32_16x16x32_f16 v[174:177], v[94:97], v[186:189], v[174:177]\n"
        "v_cvt_pk_f16_f32 v252, v236, v240\n"
        "v_cvt_pk_f16_f32 v253, v237, v241\n"
        "s_waitcnt lgkmcnt(3)\n"
        "v_mfma_f32_16x16x32_f16 v[170:173], v[66:69], v[190:193], v[170:173]\n"
        "ds_read_b128 v[214:217], v20 offset:1472\n"
        "v_mfma_f32_16x16x32_f16 v[170:173], v[70:73], v[194:197], v[170:173]\n"
        "v_mfma_f32_16x16x32_f16 v[170:173], v[74:77], v[198:201], v[170:173]\n"
        "ds_read_b128 v[186:189], v20 offset:3008\n"
        "v_mfma_f32_16x16x32_f16 v[174:177], v[98:101], v[190:193], v[174:177]\n"
        "ds_read_b128 v[190:193], v20 offset:2944\n"
        "v_mfma_f32_16x16x32_f16 v[174:177], v[102:105], v[194:197], v[174:177]\n"
        "ds_read_b128 v[194:197], v20 offset:2880\n"
        "v_mfma_f32_16x16x32_f16 v[174:177], v[106:109], v[198:201], v[174:177]\n"
        "ds_read_b128 v[198:201], v20 offset:2816\n"
        "v_mfma_f32_16x16x32_f16 v[174:177], v[242:245], v[118:121], v[174:177]\n"
        "ds_write_b128 v22, v[170:173] offset:5120\n"
        "v_mfma_f32_16x16x32_f16 v[178:181], v[242:245], v[126:129], v[178:181]\n"
        "v_mfma_f32_16x16x32_f16 v[182:185], v[242:245], v[134:137], v[182:185]\n"
        "ds_add_u32 v23, v254 offset:1024\n"
        "v_mfma_f32_16x16x32_f16 v[174:177], v[246:249], v[114:117], v[174:177]\n"
        "v_mfma_f32_16x16x32_f16 v[178:181], v[246:249], v[122:125], v[178:181]\n"
        "v_mfma_f32_16x16x32_f16 v[182:185], v[246:249], v[130:133], v[182:185]\n"
        "v_mfma_f32_16x16x32_f16 v[174:177], v[250:253], v[138:141], v[174:177]\n"
        "v_mfma_f32_16x16x32_f16 v[178:181], v[250:253], v[142:145], v[178:181]\n"
        "v_mfma_f32_16x16x32_f16 v[182:185], v[250:253], v[146:149], v[182:185]\n"
        "s_waitcnt lgkmcnt(1)\n"
        "v_mfma_f32_16x16x32_f16 v[218:221], v[202:205], v[26:29], 0\n"
        "v_mfma_f32_16x16x32_f16 v[222:225], v[202:205], v[30:33], 0\n"
        "ds_read_b128 v[202:205], v20 offset:2752\n"
        "v_mfma_f32_16x16x32_f16 v[226:229], v[206:209], v[26:29], 0\n"
        "v_mfma_f32_16x16x32_f16 v[230:233], v[206:209], v[30:33], 0\n"
        "ds_read_b128 v[206:209], v20 offset:2688\n"
        "v_mfma_f32_16x16x32_f16 v[234:237], v[210:213], v[34:37], 0\n"
        "v_mfma_f32_16x16x32_f16 v[238:241], v[214:217], v[38:41], 0\n"
        "ds_read_b128 v[210:213], v20 offset:2624\n"
        "v_mfma_f32_16x16x32_f16 v[174:177], v[46:49], v[186:189], v[174:177]\n"
        "v_mfma_f32_16x16x32_f16 v[178:181], v[78:81], v[186:189], v[178:181]\n"
        "ds_read_b128 v[214:217], v20 offset:2560\n"
        "v_mfma_f32_16x16x32_f16 v[182:185], v[110:113], v[186:189], v[182:185]\n"
        "v_cvt_pk_f16_f32 v242, v218, v222\n"
        "v_cvt_pk_f16_f32 v243, v219, v223\n"
        "v_mfma_f32_16x16x32_f16 v[174:177], v[50:53], v[190:193], v[174:177]\n"
        "ds_read_b128 v[186:189], v21 offset:2560\n"
        "v_mfma_f32_16x16x32_f16 v[178:181], v[82:85], v[190:193], v[178:181]\n"
        "v_cvt_pk_f16_f32 v244, v220, v224\n"
        "v_cvt_pk_f16_f32 v245, v221, v225\n"
        "v_mfma_f32_16x16x32_f16 v[174:177], v[54:57], v[194:197], v[174:177]\n"
        "ds_read_b128 v[190:193], v21 offset:2816\n"
        "v_mfma_f32_16x16x32_f16 v[178:181], v[86:89], v[194:197], v[178:181]\n"
        "v_cvt_pk_f16_f32 v246, v226, v230\n"
        "v_cvt_pk_f16_f32 v247, v227, v231\n"
        "v_mfma_f32_16x16x32_f16 v[174:177], v[58:61], v[198:201], v[174:177]\n"
        "v_cvt_pk_f16_f32 v248, v228, v232\n"
        "v_cvt_pk_f16_f32 v249, v229, v233\n"
        "v_mfma_f32_16x16x32_f16 v[178:181], v[90:93], v[198:201], v[178:181]\n"
        "ds_read_b128 v[194:197], v20 offset:1968\n"
        "s_waitcnt lgkmcnt(4)\n"
        "v_mfma_f32_16x16x32_f16 v[174:177], v[62:65], v[202:205], v[174:177]\n"
        "ds_read_b128 v[198:201], v20 offset:1984\n"
        "v_mfma_f32_16x16x32_f16 v[178:181], v[94:97], v[202:205], v[178:181]\n"
        "v_cvt_pk_f16_f32 v250, v234, v238\n"
        "v_cvt_pk_f16_f32 v251, v235, v239\n"
        "v_mfma_f32_16x16x32_f16 v[174:177], v[66:69], v[206:209], v[174:177]\n"
        "v_cvt_pk_f16_f32 v252, v236, v240\n"
        "v_cvt_pk_f16_f32 v253, v237, v241\n"
        "v_mfma_f32_16x16x32_f16 v[174:177], v[70:73], v[210:213], v[174:177]\n"
        "ds_read_b128 v[202:205], v20 offset:3520\n"
        "s_waitcnt lgkmcnt(4)\n"
        "v_mfma_f32_16x16x32_f16 v[174:177], v[74:77], v[214:217], v[174:177]\n"
        "v_mfma_f32_16x16x32_f16 v[178:181], v[98:101], v[206:209], v[178:181]\n"
        "ds_read_b128 v[206:209], v20 offset:3456\n"
        "v_mfma_f32_16x16x32_f16 v[178:181], v[102:105], v[210:213], v[178:181]\n"
        "ds_read_b128 v[210:213], v20 offset:3392\n"
        "v_mfma_f32_16x16x32_f16 v[178:181], v[106:109], v[214:217], v[178:181]\n"
        "ds_read_b128 v[214:217], v20 offset:3328\n"
        "v_mfma_f32_16x16x32_f16 v[178:181], v[242:245], v[118:121], v[178:181]\n"
        "s_nop 0\n"
        "ds_write_b128 v22, v[174:177] offset:6400\n"
        "v_mfma_f32_16x16x32_f16 v[182:185], v[242:245], v[126:129], v[182:185]\n"
        "v_mfma_f32_16x16x32_f16 v[178:181], v[246:249], v[114:117], v[178:181]\n"
        "ds_add_u32 v23, v254 offset:1280\n"
        "v_mfma_f32_16x16x32_f16 v[182:185], v[246:249], v[122:125], v[182:185]\n"
        "v_mfma_f32_16x16x32_f16 v[178:181], v[250:253], v[138:141], v[178:181]\n"
        "v_mfma_f32_16x16x32_f16 v[182:185], v[250:253], v[142:145], v[182:185]\n"
        "v_mfma_f32_16x16x32_f16 v[218:221], v[186:189], v[26:29], 0\n"
        "v_mfma_f32_16x16x32_f16 v[222:225], v[186:189], v[30:33], 0\n"
        "ds_read_b128 v[186:189], v20 offset:3264\n"
        "s_waitcnt lgkmcnt(3)\n"
        "v_mfma_f32_16x16x32_f16 v[226:229], v[190:193], v[26:29], 0\n"
        "v_mfma_f32_16x16x32_f16 v[230:233], v[190:193], v[30:33], 0\n"
        "ds_read_b128 v[190:193], v20 offset:3200\n"
        "v_mfma_f32_16x16x32_f16 v[234:237], v[194:197], v[34:37], 0\n"
        "ds_read_b128 v[194:197], v20 offset:3136\n"
        "v_mfma_f32_16x16x32_f16 v[238:241], v[198:201], v[38:41], 0\n"
        "ds_read_b128 v[198:201], v20 offset:3072\n"
        "v_mfma_f32_16x16x32_f16 v[178:181], v[46:49], v[202:205], v[178:181]\n"
        "v_mfma_f32_16x16x32_f16 v[182:185], v[78:81], v[202:205], v[182:185]\n"
        "ds_read_b128 v[202:205], v20 offset:4032\n"
        "v_mfma_f32_16x16x32_f16 v[178:181], v[50:53], v[206:209], v[178:181]\n"
        "v_cvt_pk_f16_f32 v242, v218, v222\n"
        "v_cvt_pk_f16_f32 v243, v219, v223\n"
        "v_mfma_f32_16x16x32_f16 v[182:185], v[82:85], v[206:209], v[182:185]\n"
        "ds_read_b128 v[206:209], v20 offset:3968\n"
        "v_mfma_f32_16x16x32_f16 v[178:181], v[54:57], v[210:213], v[178:181]\n"
        "v_cvt_pk_f16_f32 v244, v220, v224\n"
        "v_cvt_pk_f16_f32 v245, v221, v225\n"
        "v_mfma_f32_16x16x32_f16 v[182:185], v[86:89], v[210:213], v[182:185]\n"
        "s_cbranch_scc0 L_NOPOLL\n"
        "global_load_dwordx4 v[78:81], v9, s[28:29] sc1\n"
        "global_load_dwordx4 v[82:85], v24, s[28:29] sc1\n"
        "global_load_dwordx4 v[86:89], v25, s[28:29] sc1\n"
        "L_NOPOLL:\n"
        "ds_read_b128 v[210:213], v20 offset:3904\n"
        "v_mfma_f32_16x16x32_f16 v[178:181], v[58:61], v[214:217], v[178:181]\n"
        "v_cvt_pk_f16_f32 v246, v226, v230\n"
        "v_cvt_pk_f16_f32 v247, v227, v231\n"
        "v_mfma_f32_16x16x32_f16 v[182:185], v[90:93], v[214:217], v[182:185]\n"
        "ds_read_b128 v[214:217], v20 offset:3840\n"
        "s_waitcnt lgkmcnt(4)\n"
        "v_mfma_f32_16x16x32_f16 v[178:181], v[62:65], v[186:189], v[178:181]\n"
        "v_cvt_pk_f16_f32 v248, v228, v232\n"
        "v_cvt_pk_f16_f32 v249, v229, v233\n"
        "v_mfma_f32_16x16x32_f16 v[182:185], v[94:97], v[186:189], v[182:185]\n"
        "ds_read_b128 v[186:189], v20 offset:3776\n"
        "v_mfma_f32_16x16x32_f16 v[178:181], v[66:69], v[190:193], v[178:181]\n"
        "v_cvt_pk_f16_f32 v250, v234, v238\n"
        "v_cvt_pk_f16_f32 v251, v235, v239\n"
        "v_mfma_f32_16x16x32_f16 v[178:181], v[70:73], v[194:197], v[178:181]\n"
        "v_cvt_pk_f16_f32 v252, v236, v240\n"
        "v_cvt_pk_f16_f32 v253, v237, v241\n"
        "v_mfma_f32_16x16x32_f16 v[178:181], v[74:77], v[198:201], v[178:181]\n"
        "v_mfma_f32_16x16x32_f16 v[182:185], v[98:101], v[190:193], v[182:185]\n"
        "ds_read_b128 v[190:193], v20 offset:3712\n"
        "v_mfma_f32_16x16x32_f16 v[182:185], v[102:105], v[194:197], v[182:185]\n"
        "ds_read_b128 v[194:197], v20 offset:3648\n"
        "v_mfma_f32_16x16x32_f16 v[182:185], v[106:109], v[198:201], v[182:185]\n"
        "ds_read_b128 v[198:201], v20 offset:3584\n"
        "v_mfma_f32_16x16x32_f16 v[182:185], v[242:245], v[118:121], v[182:185]\n"
        "s_nop 0\n"
        "ds_write_b128 v22, v[178:181] offset:7680\n"
        "v_mfma_f32_16x16x32_f16 v[182:185], v[246:249], v[114:117], v[182:185]\n"
        "v_mfma_f32_16x16x32_f16 v[182:185], v[250:253], v[138:141], v[182:185]\n"
        "ds_add_u32 v23, v254 offset:1536\n"
        "s_waitcnt lgkmcnt(5)\n"
        "v_mfma_f32_16x16x32_f16 v[182:185], v[46:49], v[202:205], v[182:185]\n"
        "v_mfma_f32_16x16x32_f16 v[182:185], v[50:53], v[206:209], v[182:185]\n"
        "v_mfma_f32_16x16x32_f16 v[182:185], v[54:57], v[210:213], v[182:185]\n"
        "v_mfma_f32_16x16x32_f16 v[182:185], v[58:61], v[214:217], v[182:185]\n"
        "v_mfma_f32_16x16x32_f16 v[182:185], v[62:65], v[186:189], v[182:185]\n"
        "s_waitcnt lgkmcnt(2)\n"
        "v_mfma_f32_16x16x32_f16 v[182:185], v[66:69], v[190:193], v[182:185]\n"
        "v_mfma_f32_16x16x32_f16 v[182:185], v[70:73], v[194:197], v[182:185]\n"
        "v_mfma_f32_16x16x32_f16 v[182:185], v[74:77], v[198:201], v[182:185]\n"
        "s_nop 7\n"
        "ds_write_b128 v22, v[182:185] offset:8960\n"
        "ds_add_u32 v23, v254 offset:1792\n"
        "s_cbranch_scc1 L_FCHK\n"
        "s_waitcnt lgkmcnt(0)\n"
        "s_barrier\n"
        "s_endpgm\n"
        "L_FPOLL:\n"
        "s_waitcnt vmcnt(2)\n"
        "global_load_dwordx4 v[78:81], v9, s[28:29] sc1\n"
        "s_waitcnt vmcnt(2)\n"
        "global_load_dwordx4 v[82:85], v24, s[28:29] sc1\n"
        "s_waitcnt vmcnt(2)\n"
        "global_load_dwordx4 v[86:89], v25, s[28:29] sc1\n"
        "L_FCHK:\n"
        "v_lshrrev_b32 v4, 5, v1\n"
        "v_lshl_add_u32 v67, v5, 1, v4\n"
        "s_waitcnt vmcnt(2)\n"
        "v_xor_b32 v66, s58, v79\n"
        "v_bitop3_b32 v66, v81, s58, v66 bitop3:0xbe\n"
        "v_lshrrev_b32 v66, 16, v66\n"
        "v_and_b32 v68, 0x7ff, v78\n"
        "v_min_u32 v66, 255, v66\n"
        "s_waitcnt vmcnt(1)\n"
        "v_xor_b32 v82, s56, v82\n"
        "v_xor_b32 v83, s57, v83\n"
        "v_or3_b32 v66, v82, v83, v66\n"
        "v_xor_b32 v84, s56, v84\n"
        "v_xor_b32 v85, s57, v85\n"
        "v_or3_b32 v66, v84, v85, v66\n"
        "s_waitcnt vmcnt(0)\n"
        "v_xor_b32 v86, s56, v86\n"
        "v_xor_b32 v87, s57, v87\n"
        "v_or3_b32 v66, v86, v87, v66\n"
        "v_xor_b32 v88, s56, v88\n"
        "v_xor_b32_e64 v89, s57, v89\n"
        "v_or3_b32 v66, v88, v89, v66\n"
        "v_and_b32 v66, 0xff, v66\n"
        "v_cmp_ne_u32 vcc, 0, v66\n"
        "s_sub_u32 s47, s47, 1\n"
        "s_cbranch_vccz L_FOK\n"
        "s_cbranch_scc0 L_FPOLL\n"
        "L_FOK:\n"
        "v_mul_u32_u24 v4, 4608, v67\n"
        "v_lshl_add_u32 v4, v68, 1, v4\n"
        "ds_read_u16 v70, v4 offset:50688\n"
        "v_lshlrev_b32_e64 v4, 1, v67\n"
        "v_lshlrev_b32_e64 v71, 16, v80\n"
        "v_and_b32 v80, 0xffff0000, v80\n"
        "v_lshlrev_b32_e64 v6, 2, v67\n"
        "v_add_u32 v6, 111616, v6\n"
        "ds_read_b32 v72, v6\n"
        "s_mov_b64 exec, 0x3fffff\n"
        "v_pk_add_f32 v[82:83], v[82:83], v[86:87]\n"
        "v_pk_add_f32 v[84:85], v[84:85], v[88:89]\n"
        "s_mov_b64 exec, -1\n"
        "v_and_b32 v74, 0xffff0000, v78\n"
        "v_mul_f32 v74, 0xbfb8aa3b, v74\n"
        "v_permlane32_swap_b32 v82, v84\n"
        "v_permlane32_swap_b32 v83, v85\n"
        "v_exp_f32 v74, v74\n"
        "v_pk_add_f32 v[82:83], v[82:83], v[84:85]\n"
        "v_lshl_add_u32 v4, v68, 10, v4\n"
        "v_add_f32 v74, 1.0, v74\n"
        "v_permlane16_swap_b32 v82, v83\n"
        "v_rcp_f32 v74, v74\n"
        "v_add_f32 v82, v82, v83\n"
        "v_lshrrev_b32_e64 v6, 8, v68\n"
        "v_mul_u32_u24 v6, 1280, v6\n"
        "v_add_f32_dpp v82, v82, v82 row_ror:8 row_mask:0xf bank_mask:0xf\n"
        "v_bfe_u32 v7, v68, 2, 2\n"
        "s_mov_b32 s38, 320\n"
        "v_mad_u32_u24 v6, v7, s38, v6\n"
        "v_add_f32_dpp v82, v82, v82 row_ror:4 row_mask:0xf bank_mask:0xf\n"
        "v_and_b32 v7, 0xf0, v68\n"
        "v_and_b32_e64 v8, 3, v68\n"
        "v_add_f32_dpp v82, v82, v82 row_ror:2 row_mask:0xf bank_mask:0xf\n"
        "v_lshl_add_u32 v7, v8, 2, v7\n"
        "s_mov_b32 s38, 10240\n"
        "v_mad_u32_u24 v6, v67, s38, v6\n"
        "v_cvt_pk_bf16_f32 v82, v82, v74\n"
        "s_mov_b32 s38, 68608\n"
        "v_add3_u32 v73, v6, v7, s38\n"
        "v_and_b32 v74, 0xffff0000, v82\n"
        "v_lshlrev_b32 v82, 16, v82\n"
        "v_mul_f32 v82, v82, v74\n"
        "s_waitcnt lgkmcnt(1)\n"
        "v_cvt_f32_f16 v70, v70\n"
        "v_cvt_pk_bf16_f32 v82, v82, v82\n"
        "v_mov_b32 v75, s42\n"
        "v_lshlrev_b32 v82, 16, v82\n"
        "v_add_f32 v82, v82, v70\n"
        "v_mul_f32 v74, 0xbfb8aa3b, v82\n"
        "v_mov_b32 v6, s43\n"
        "v_exp_f32_e64 v74, v74\n"
        "s_mov_b32 s38, 0xaaaaaaaa\n"
        "s_mov_b32 s39, s38\n"
        "v_cndmask_b32 v75, v75, v6, s[38:39]\n"
        "v_add_f32 v74, 1.0, v74\n"
        "s_mov_b32 exec_hi, 0\n"
        "v_lshlrev_b32 v75, 16, v75\n"
        "s_mov_b32 exec_hi, -1\n"
        "v_rcp_f32 v74, v74\n"
        "v_and_b32 v75, 0xffff0000, v75\n"
        "v_fmac_f32 v71, v70, v80\n"
        "v_cvt_pk_bf16_f32 v71, v71, v71\n"
        "v_lshlrev_b32 v71, 16, v71\n"
        "v_mul_f32 v82, v82, v74\n"
        "v_mul_f32 v75, v75, v70\n"
        "s_lshl_b32 s38, s33, 3\n"
        "v_add_u32 v4, s38, v4\n"
        "s_waitcnt lgkmcnt(0)\n"
        "s_barrier\n"
        "ds_read_b32 v73, v73\n"
        "s_mov_b32 exec_lo, 0x00030003\n"
        "s_mov_b32 exec_hi, 0x00030003\n"
        "s_waitcnt lgkmcnt(0)\n"
        "v_fmac_f32 v75, v73, v72\n"
        "v_fmac_f32_e64 v71, v75, v82\n"
        "v_cvt_pk_bf16_f32 v71, v71, v71\n"
        "global_store_short v4, v71, s[26:27]\n"
        "s_endpgm\n"
        ".fill 11, 4, 0xbf800000\n"
        "L_E:\n"
        "s_lshl2_add_u32 s38, s33, s34\n"
        "s_lshl_b32 s38, s38, 8\n"
        "v_lshl_add_u32 v3, v1, 2, s38\n"
        "s_lshl3_add_u32 s35, s32, s30\n"
        "s_mul_i32 s38, s35, 12288\n"
        "s_lshl_b32 s39, s31, 7\n"
        "s_add_u32 s38, s38, s39\n"
        "v_lshl_add_u32 v7, v1, 10, s38\n"
        "s_lshl_b32 s39, s33, 3\n"
        "v_and_b32 v6, 0xff, v0\n"
        "v_lshl_add_u32 v10, v6, 11, s39\n"
        "s_waitcnt lgkmcnt(0)\n"
        "global_load_dword v244, v3, s[6:7]\n"
        "global_load_dword v245, v3, s[8:9]\n"
        "global_load_dword v246, v3, s[10:11]\n"
        "s_nop 0\n"
        "global_load_dword v247, v3, s[12:13]\n"
        "s_mov_b64 exec, 0xfff\n"
        "global_load_dword v253, v7, s[4:5]\n"
        "s_load_dwordx2 s[62:63], s[14:15], s39\n"
        "s_load_dwordx2 s[42:43], s[16:17], s39\n"
        "s_load_dword s40, s[28:29], 0x0\n"
        "s_add_u32 s48, s4, 524288\n"
        "s_addc_u32 s49, s5, 0\n"
        "s_mov_b64 exec, -1\n"
        "s_barrier\n"
        "global_load_dwordx2 v[226:227], v10, s[4:5] offset:0\n"
        "global_load_dwordx2 v[228:229], v10, s[4:5] offset:1024\n"
        "s_nop 0\n"
        "global_load_dwordx2 v[230:231], v10, s[48:49] offset:0\n"
        "global_load_dwordx2 v[232:233], v10, s[48:49] offset:1024\n"
        "s_add_u32 s50, s4, 1048576\n"
        "s_addc_u32 s51, s5, 0\n"
        "s_add_u32 s52, s4, 1572864\n"
        "s_addc_u32 s53, s5, 0\n"
        "v_and_b32 v2, 15, v1\n"
        "v_and_b32_e64 v5, 48, v1\n"
        "s_mul_i32 s38, s30, 4608\n"
        "s_mov_b32 m0, s38\n"
        "v_mul_u32_u24 v4, 288, v2\n"
        "v_add3_u32 v13, v4, v5, s38\n"
        "s_mul_i32 s39, s34, 3072\n"
        "s_add_u32 s39, s39, 68608\n"
        "v_lshl_add_u32 v14, v1, 4, s39\n"
        "s_lshl2_add_u32 s39, s34, 111616\n"
        "v_mov_b32 v15, s39\n"
        "v_lshlrev_b32 v4, 4, v6\n"
        "v_add_u32 v11, 8192, v4\n"
        "v_add_u32 v12, 16384, v4\n"
        "v_lshlrev_b32_e64 v4, 2, v6\n"
        "v_add_u32 v17, 50688, v4\n"
        "s_bfe_u32 s59, s30, 0x10001\n"
        "s_mul_i32 s39, s59, 1280\n"
        "s_add_u32 s39, s39, 68608\n"
        "v_bfe_u32 v4, v6, 1, 2\n"
        "v_mul_u32_u24 v4, 320, v4\n"
        "v_bfe_u32 v7, v6, 3, 4\n"
        "v_lshl_add_u32 v4, v7, 4, v4\n"
        "v_and_b32 v7, 1, v6\n"
        "v_lshl_add_u32 v4, v7, 3, v4\n"
        "v_add_u32 v18, s39, v4\n"
        "s_lshl_b32 s39, s59, 8\n"
        "s_add_u32 s39, s39, 109568\n"
        "v_mov_b32 v19, s39\n"
        "v_mov_b32 v20, 111616\n"
        "v_mov_b32 v248, 0x3fb8aa3b\n"
        "s_lshl_b32 s38, s34, 4\n"
        "s_waitcnt lgkmcnt(0)\n"
        "s_lshr_b64 s[62:63], s[62:63], s38\n"
        "s_lshl_b32 s62, s62, 16\n"
        "v_mul_f32 v46, s62, v248\n"
        "s_waitcnt vmcnt(8)\n"
        "v_and_b32 v47, 0xffff0000, v244\n"
        "s_setprio 1\n"
        "v_mul_f32 v47, v248, v47\n"
        "v_exp_f32 v46, v46\n"
        "v_exp_f32 v47, v47\n"
        "s_waitcnt vmcnt(6)\n"
        "v_and_b32 v56, 0xffff0000, v246\n"
        "s_waitcnt vmcnt(5)\n"
        "v_and_b32 v57, 0xffff0000, v247\n"
        "v_lshlrev_b32 v58, 16, v246\n"
        "v_lshlrev_b32 v59, 16, v247\n"
        "v_cvt_pk_bf16_f32 v46, v46, v47\n"
        "v_max3_f32 v60, |v56|, |v57|, |v58|\n"
        "v_and_b32 v49, 0xffff0000, v245\n"
        "v_max_f32_e64 v60, v60, |v59|\n"
        "v_and_b32 v48, 0xffff0000, v46\n"
        "v_lshlrev_b32_e64 v46, 16, v46\n"
        "v_max_u32_dpp v60, v60, v60 row_shr:1 row_mask:0xf bank_mask:0xf bound_ctrl:1\n"
        "v_mul_f32 v50, 0xbfb8aa3b, v46\n"
        "v_mul_f32 v51, 0.15915494, v46\n"
        "v_max_u32_dpp v60, v60, v60 row_shr:2 row_mask:0xf bank_mask:0xf bound_ctrl:1\n"
        "v_pk_mul_f32 v[52:53], v[48:49], v[50:51]\n"
        "v_readfirstlane_b32 s41, v46\n"
        "v_max_u32_dpp v60, v60, v60 row_shr:4 row_mask:0xf bank_mask:0xf bound_ctrl:1\n"
        "v_exp_f32 v90, v52\n"
        "v_fract_f32 v84, v53\n"
        "v_max_u32_dpp v60, v60, v60 row_shr:8 row_mask:0xf bank_mask:0xf bound_ctrl:1\n"
        "v_cos_f32 v62, v84\n"
        "v_sin_f32 v63, v84\n"
        "v_max_u32_dpp v60, v60, v60 row_bcast:15 row_mask:0xa bank_mask:0xf\n"
        "v_mul_f32 v54, 0x42800000, v52\n"
        "v_mul_f32 v55, 0x42800000, v53\n"
        "v_max_u32_dpp v60, v60, v60 row_bcast:31 row_mask:0xc bank_mask:0xf\n"
        "v_pk_mul_f32 v[62:63], v[62:63], v[90:91] op_sel_hi:[1,0]\n"
        "v_exp_f32_e64 v91, v54\n"
        "v_fract_f32_e64 v86, v55\n"
        "v_readlane_b32 s44, v60, 63\n"
        "v_pk_mul_f32 v[84:85], v[62:63], v[62:63] op_sel_hi:[1,0]\n"
        "v_cos_f32 v74, v86\n"
        "v_pk_fma_f32 v[64:65], v[62:63], v[62:63], v[84:85] op_sel:[1,1,0] op_sel_hi:[0,1,1] neg_lo:[0,1,0]\n"
        "v_sin_f32_e64 v75, v86\n"
        "s_lshr_b32 s44, s44, 23\n"
        "s_max_u32 s44, s44, 64\n"
        "s_min_u32 s44, s44, 190\n"
        "s_lshl_b32 s44, s44, 23\n"
        "s_sub_u32 s45, 0x7f000000, s44\n"
        "v_pk_mul_f32 v[84:85], v[64:65], v[64:65] op_sel_hi:[1,0]\n"
        "v_pk_mul_f32 v[74:75], v[74:75], v[90:91] op_sel:[0,1]\n"
        "v_pk_fma_f32 v[66:67], v[64:65], v[64:65], v[84:85] op_sel:[1,1,0] op_sel_hi:[0,1,1] neg_lo:[0,1,0]\n"
        "v_mul_f32 v56, s45, v56\n"
        "v_mul_f32 v57, s45, v57\n"
        "v_pk_mul_f32 v[84:85], v[66:67], v[66:67] op_sel_hi:[1,0]\n"
        "global_load_dwordx2 v[234:235], v10, s[50:51] offset:0\n"
        "v_pk_mul_f32 v[86:87], v[74:75], v[74:75] op_sel_hi:[1,0]\n"
        "v_pk_mul_f32 v[88:89], v[56:57], v[62:63] op_sel_hi:[1,0]\n"
        "v_pk_fma_f32 v[68:69], v[66:67], v[66:67], v[84:85] op_sel:[1,1,0] op_sel_hi:[0,1,1] neg_lo:[0,1,0]\n"
        "v_pk_fma_f32 v[76:77], v[74:75], v[74:75], v[86:87] op_sel:[1,1,0] op_sel_hi:[0,1,1] neg_lo:[0,1,0]\n"
        "v_pk_fma_f32 v[92:93], v[56:57], v[62:63], v[88:89] op_sel:[1,1,0] op_sel_hi:[0,1,1] neg_lo:[0,1,0]\n"
        "v_pk_mul_f32 v[84:85], v[68:69], v[68:69] op_sel_hi:[1,0]\n"
        "v_pk_mul_f32 v[86:87], v[76:77], v[76:77] op_sel_hi:[1,0]\n"
        "v_pk_mul_f32 v[88:89], v[56:57], v[64:65] op_sel_hi:[1,0]\n"
        "v_pk_fma_f32 v[70:71], v[68:69], v[68:69], v[84:85] op_sel:[1,1,0] op_sel_hi:[0,1,1] neg_lo:[0,1,0]\n"
        "v_pk_fma_f32 v[78:79], v[76:77], v[76:77], v[86:87] op_sel:[1,1,0] op_sel_hi:[0,1,1] neg_lo:[0,1,0]\n"
        "v_pk_fma_f32 v[94:95], v[56:57], v[64:65], v[88:89] op_sel:[1,1,0] op_sel_hi:[0,1,1] neg_lo:[0,1,0]\n"
        "v_pk_mul_f32 v[84:85], v[70:71], v[70:71] op_sel_hi:[1,0]\n"
        "v_pk_mul_f32 v[86:87], v[68:69], v[70:71] op_sel_hi:[1,0]\n"
        "v_pk_mul_f32 v[88:89], v[56:57], v[66:67] op_sel_hi:[1,0]\n"
        "v_pk_fma_f32 v[72:73], v[70:71], v[70:71], v[84:85] op_sel:[1,1,0] op_sel_hi:[0,1,1] neg_lo:[0,1,0]\n"
        "v_pk_fma_f32 v[108:109], v[68:69], v[70:71], v[86:87] op_sel:[1,1,0] op_sel_hi:[0,1,1] neg_lo:[0,1,0]\n"
        "v_pk_fma_f32 v[98:99], v[56:57], v[66:67], v[88:89] op_sel:[1,1,0] op_sel_hi:[0,1,1] neg_lo:[0,1,0]\n"
        "v_mov_b32 v128, 0x3c00\n"
        "v_cvt_pk_f16_f32 v126, v74, -v75\n"
        "ds_write_addtid_b32 v128 offset:0\n"
        "ds_write_addtid_b32 v126 offset:2304\n"
        "v_pk_mul_f32 v[84:85], v[72:73], v[74:75] op_sel_hi:[1,0]\n"
        "v_pk_mul_f32 v[86:87], v[56:57], v[68:69] op_sel_hi:[1,0]\n"
        "v_pk_fma_f32 v[110:111], v[72:73], v[74:75], v[84:85] op_sel:[1,1,0] op_sel_hi:[0,1,1] neg_lo:[0,1,0]\n"
        "v_pk_fma_f32 v[106:107], v[56:57], v[68:69], v[86:87] op_sel:[1,1,0] op_sel_hi:[0,1,1] neg_lo:[0,1,0]\n"
        "v_cvt_pk_f16_f32 v122, v68, -v69\n"
        "v_cvt_pk_f16_f32 v123, v70, -v71\n"
        "ds_write_addtid_b32 v122 offset:288\n"
        "ds_write_addtid_b32 v123 offset:576\n"
        "v_pk_mul_f32 v[84:85], v[78:79], v[78:79] op_sel_hi:[1,0]\n"
        "v_pk_mul_f32 v[86:87], v[92:93], v[64:65] op_sel_hi:[1,0]\n"
        "v_pk_fma_f32 v[80:81], v[78:79], v[78:79], v[84:85] op_sel:[1,1,0] op_sel_hi:[0,1,1] neg_lo:[0,1,0]\n"
        "v_pk_fma_f32 v[96:97], v[92:93], v[64:65], v[86:87] op_sel:[1,1,0] op_sel_hi:[0,1,1] neg_lo:[0,1,0]\n"
        "v_cvt_pk_f16_f32 v124, v108, -v109\n"
        "v_cvt_pk_f16_f32 v125, v72, -v73\n"
        "ds_write_addtid_b32 v124 offset:864\n"
        "ds_write_addtid_b32 v125 offset:1152\n"
        "v_pk_mul_f16 v132, v122, v126 op_sel_hi:[1,0]\n"
        "v_pk_mul_f16 v133, v123, v126 op_sel_hi:[1,0]\n"
        "v_pk_fma_f16 v132, v122, v126, v132 op_sel:[1,1,0] op_sel_hi:[0,1,1] neg_lo:[0,1,0]\n"
        "v_pk_fma_f16 v133, v123, v126, v133 op_sel:[1,1,0] op_sel_hi:[0,1,1] neg_lo:[0,1,0]\n"
        "ds_write_addtid_b32 v132 offset:2592\n"
        "ds_write_addtid_b32 v133 offset:2880\n"
        "v_pk_mul_f32 v[84:85], v[92:93], v[66:67] op_sel_hi:[1,0]\n"
        "v_pk_mul_f32 v[86:87], v[94:95], v[66:67] op_sel_hi:[1,0]\n"
        "v_pk_fma_f32 v[100:101], v[92:93], v[66:67], v[84:85] op_sel:[1,1,0] op_sel_hi:[0,1,1] neg_lo:[0,1,0]\n"
        "v_pk_fma_f32 v[102:103], v[94:95], v[66:67], v[86:87] op_sel:[1,1,0] op_sel_hi:[0,1,1] neg_lo:[0,1,0]\n"
        "v_cvt_pk_f16_f32 v127, v110, -v111\n"
        "v_pk_mul_f16 v128, v124, v126 op_sel_hi:[1,0]\n"
        "v_pk_fma_f16 v128, v124, v126, v128 op_sel:[1,1,0] op_sel_hi:[0,1,1] neg_lo:[0,1,0]\n"
        "global_load_dwordx2 v[236:237], v10, s[50:51] offset:1024\n"
        "ds_write_addtid_b32 v127 offset:3456\n"
        "ds_write_addtid_b32 v128 offset:3168\n"
        "v_pk_mul_f16 v134, v122, v125 op_sel_hi:[1,0]\n"
        "v_pk_mul_f16 v135, v123, v125 op_sel_hi:[1,0]\n"
        "v_pk_fma_f16 v134, v122, v125, v134 op_sel:[1,1,0] op_sel_hi:[0,1,1] neg_lo:[0,1,0]\n"
        "v_pk_fma_f16 v135, v123, v125, v135 op_sel:[1,1,0] op_sel_hi:[0,1,1] neg_lo:[0,1,0]\n"
        "ds_write_addtid_b32 v134 offset:1440\n"
        "ds_write_addtid_b32 v135 offset:1728\n"
        "v_pk_mul_f32 v[84:85], v[96:97], v[66:67] op_sel_hi:[1,0]\n"
        "v_pk_mul_f32 v[86:87], v[76:77], v[78:79] op_sel_hi:[1,0]\n"
        "v_pk_fma_f32 v[104:105], v[96:97], v[66:67], v[84:85] op_sel:[1,1,0] op_sel_hi:[0,1,1] neg_lo:[0,1,0]\n"
        "v_pk_fma_f32 v[112:113], v[76:77], v[78:79], v[86:87] op_sel:[1,1,0] op_sel_hi:[0,1,1] neg_lo:[0,1,0]\n"
        "v_pk_mul_f16 v132, v124, v125 op_sel_hi:[1,0]\n"
        "v_pk_mul_f16 v133, v122, v127 op_sel_hi:[1,0]\n"
        "v_pk_fma_f16 v132, v124, v125, v132 op_sel:[1,1,0] op_sel_hi:[0,1,1] neg_lo:[0,1,0]\n"
        "v_pk_fma_f16 v133, v122, v127, v133 op_sel:[1,1,0] op_sel_hi:[0,1,1] neg_lo:[0,1,0]\n"
        "ds_write_addtid_b32 v132 offset:2016\n"
        "ds_write_addtid_b32 v133 offset:3744\n"
        "v_pk_mul_f16 v134, v123, v127 op_sel_hi:[1,0]\n"
        "v_pk_mul_f16 v135, v124, v127 op_sel_hi:[1,0]\n"
        "v_pk_fma_f16 v134, v123, v127, v134 op_sel:[1,1,0] op_sel_hi:[0,1,1] neg_lo:[0,1,0]\n"
        "v_pk_fma_f16 v135, v124, v127, v135 op_sel:[1,1,0] op_sel_hi:[0,1,1] neg_lo:[0,1,0]\n"
        "ds_write_addtid_b32 v134 offset:4032\n"
        "ds_write_addtid_b32 v135 offset:4320\n"
        "ds_read_b128 v[140:143], v13 offset:0\n"
        "ds_read_b128 v[144:147], v13 offset:64\n"
        "ds_read_b128 v[148:151], v13 offset:128\n"
        "ds_read_b128 v[152:155], v13 offset:192\n"
        "v_pk_mul_f32 v[84:85], v[80:81], v[80:81] op_sel_hi:[1,0]\n"
        "v_pk_mul_f32 v[86:87], v[106:107], v[80:81] op_sel_hi:[1,0]\n"
        "v_pk_fma_f32 v[82:83], v[80:81], v[80:81], v[84:85] op_sel:[1,1,0] op_sel_hi:[0,1,1] neg_lo:[0,1,0]\n"
        "v_pk_fma_f32 v[116:117], v[106:107], v[80:81], v[86:87] op_sel:[1,1,0] op_sel_hi:[0,1,1] neg_lo:[0,1,0]\n"
        "v_cvt_pk_f16_f32 v122, v92, v93\n"
        "v_cvt_pk_f16_f32 v123, v94, v95\n"
        "ds_write_addtid_b32 v122 offset:0\n"
        "ds_write_addtid_b32 v123 offset:288\n"
        "v_cvt_pk_f16_f32 v136, v76, v77\n"
        "v_pk_mul_f32 v[84:85], v[98:99], v[80:81] op_sel_hi:[1,0]\n"
        "v_pk_fma_f32 v[114:115], v[98:99], v[80:81], v[84:85] op_sel:[1,1,0] op_sel_hi:[0,1,1] neg_lo:[0,1,0]\n"
        "v_cvt_pk_f16_f32 v124, v96, v97\n"
        "v_cvt_pk_f16_f32 v125, v98, v99\n"
        "ds_write_addtid_b32 v124 offset:576\n"
        "ds_write_addtid_b32 v125 offset:864\n"
        "v_pk_mul_f32 v[84:85], v[106:107], v[82:83] op_sel_hi:[1,0]\n"
        "v_pk_mul_f32 v[86:87], v[116:117], v[82:83] op_sel_hi:[1,0]\n"
        "v_pk_fma_f32 v[118:119], v[106:107], v[82:83], v[84:85] op_sel:[1,1,0] op_sel_hi:[0,1,1] neg_lo:[0,1,0]\n"
        "v_pk_fma_f32 v[120:121], v[116:117], v[82:83], v[86:87] op_sel:[1,1,0] op_sel_hi:[0,1,1] neg_lo:[0,1,0]\n"
        "v_cvt_pk_f16_f32 v126, v100, v101\n"
        "v_cvt_pk_f16_f32 v127, v102, v103\n"
        "ds_write_addtid_b32 v126 offset:1152\n"
        "global_load_dwordx2 v[238:239], v10, s[52:53] offset:0\n"
        "s_setprio 0\n"
        "s_nop 0\n"
        "ds_write_addtid_b32 v127 offset:1440\n"
        "v_pk_mul_f16 v132, v122, v136 op_sel_hi:[1,0]\n"
        "v_pk_mul_f16 v133, v123, v136 op_sel_hi:[1,0]\n"
        "v_pk_fma_f16 v132, v122, v136, v132 op_sel:[1,1,0] op_sel_hi:[0,1,1] neg_lo:[0,1,0]\n"
        "v_pk_fma_f16 v133, v123, v136, v133 op_sel:[1,1,0] op_sel_hi:[0,1,1] neg_lo:[0,1,0]\n"
        "ds_write_addtid_b32 v132 offset:2304\n"
        "ds_write_addtid_b32 v133 offset:2592\n"
        "v_cvt_pk_f16_f32 v137, v78, v79\n"
        "v_cvt_pk_f16_f32 v138, v112, v113\n"
        "v_cvt_pk_f16_f32 v128, v104, v105\n"
        "v_cvt_pk_f16_f32 v129, v106, v107\n"
        "ds_write_addtid_b32 v128 offset:1728\n"
        "ds_write_addtid_b32 v129 offset:2016\n"
        "v_pk_mul_f16 v134, v124, v136 op_sel_hi:[1,0]\n"
        "v_pk_mul_f16 v135, v125, v136 op_sel_hi:[1,0]\n"
        "v_pk_fma_f16 v134, v124, v136, v134 op_sel:[1,1,0] op_sel_hi:[0,1,1] neg_lo:[0,1,0]\n"
        "v_pk_fma_f16 v135, v125, v136, v135 op_sel:[1,1,0] op_sel_hi:[0,1,1] neg_lo:[0,1,0]\n"
        "ds_write_addtid_b32 v134 offset:2880\n"
        "ds_write_addtid_b32 v135 offset:3168\n"
        "v_pk_mul_f16 v132, v126, v136 op_sel_hi:[1,0]\n"
        "v_pk_mul_f16 v133, v127, v136 op_sel_hi:[1,0]\n"
        "v_pk_fma_f16 v132, v126, v136, v132 op_sel:[1,1,0] op_sel_hi:[0,1,1] neg_lo:[0,1,0]\n"
        "v_pk_fma_f16 v133, v127, v136, v133 op_sel:[1,1,0] op_sel_hi:[0,1,1] neg_lo:[0,1,0]\n"
        "ds_write_addtid_b32 v132 offset:3456\n"
        "ds_write_addtid_b32 v133 offset:3744\n"
        "v_pk_mul_f16 v134, v128, v136 op_sel_hi:[1,0]\n"
        "v_pk_mul_f16 v135, v129, v136 op_sel_hi:[1,0]\n"
        "v_pk_fma_f16 v134, v128, v136, v134 op_sel:[1,1,0] op_sel_hi:[0,1,1] neg_lo:[0,1,0]\n"
        "v_pk_fma_f16 v135, v129, v136, v135 op_sel:[1,1,0] op_sel_hi:[0,1,1] neg_lo:[0,1,0]\n"
        "ds_write_addtid_b32 v134 offset:4032\n"
        "ds_write_addtid_b32 v135 offset:4320\n"
        "ds_read_b128 v[156:159], v13 offset:0\n"
        "ds_read_b128 v[160:163], v13 offset:64\n"
        "ds_read_b128 v[164:167], v13 offset:128\n"
        "ds_read_b128 v[168:171], v13 offset:192\n"
        "v_pk_mul_f16 v132, v123, v137 op_sel_hi:[1,0]\n"
        "v_pk_mul_f16 v133, v125, v137 op_sel_hi:[1,0]\n"
        "v_pk_fma_f16 v132, v123, v137, v132 op_sel:[1,1,0] op_sel_hi:[0,1,1] neg_lo:[0,1,0]\n"
        "v_pk_fma_f16 v133, v125, v137, v133 op_sel:[1,1,0] op_sel_hi:[0,1,1] neg_lo:[0,1,0]\n"
        "ds_write_addtid_b32 v132 offset:0\n"
        "ds_write_addtid_b32 v133 offset:288\n"
        "v_pk_mul_f16 v134, v127, v137 op_sel_hi:[1,0]\n"
        "v_pk_mul_f16 v135, v129, v137 op_sel_hi:[1,0]\n"
        "v_pk_fma_f16 v134, v127, v137, v134 op_sel:[1,1,0] op_sel_hi:[0,1,1] neg_lo:[0,1,0]\n"
        "v_pk_fma_f16 v135, v129, v137, v135 op_sel:[1,1,0] op_sel_hi:[0,1,1] neg_lo:[0,1,0]\n"
        "ds_write_addtid_b32 v134 offset:576\n"
        "ds_write_addtid_b32 v135 offset:864\n"
        "v_cvt_pk_f16_f32 v130, v114, v115\n"
        "v_cvt_pk_f16_f32 v131, v116, v117\n"
        "ds_write_addtid_b32 v130 offset:2304\n"
        "global_load_dwordx2 v[240:241], v10, s[52:53] offset:1024\n"
        "ds_write_addtid_b32 v131 offset:2592\n"
        "s_waitcnt lgkmcnt(9)\n"
        "v_mfma_f32_16x16x32_f16 v[186:189], v[156:159], v[140:143], 0\n"
        "v_pk_mul_f16 v132, v123, v138 op_sel_hi:[1,0]\n"
        "v_pk_mul_f16 v133, v125, v138 op_sel_hi:[1,0]\n"
        "v_pk_fma_f16 v132, v123, v138, v132 op_sel:[1,1,0] op_sel_hi:[0,1,1] neg_lo:[0,1,0]\n"
        "v_pk_fma_f16 v133, v125, v138, v133 op_sel:[1,1,0] op_sel_hi:[0,1,1] neg_lo:[0,1,0]\n"
        "ds_write_addtid_b32 v132 offset:1152\n"
        "ds_write_addtid_b32 v133 offset:1440\n"
        "s_waitcnt lgkmcnt(10)\n"
        "v_mfma_f32_16x16x32_f16 v[186:189], v[160:163], v[144:147], v[186:189]\n"
        "v_pk_mul_f16 v134, v127, v138 op_sel_hi:[1,0]\n"
        "v_pk_mul_f16 v135, v129, v138 op_sel_hi:[1,0]\n"
        "v_pk_fma_f16 v134, v127, v138, v134 op_sel:[1,1,0] op_sel_hi:[0,1,1] neg_lo:[0,1,0]\n"
        "v_pk_fma_f16 v135, v129, v138, v135 op_sel:[1,1,0] op_sel_hi:[0,1,1] neg_lo:[0,1,0]\n"
        "ds_write_addtid_b32 v134 offset:1728\n"
        "ds_write_addtid_b32 v135 offset:2016\n"
        "s_waitcnt lgkmcnt(11)\n"
        "v_mfma_f32_16x16x32_f16 v[186:189], v[164:167], v[148:151], v[186:189]\n"
        "v_pk_mul_f16 v132, v130, v136 op_sel_hi:[1,0]\n"
        "v_pk_mul_f16 v133, v131, v136 op_sel_hi:[1,0]\n"
        "v_pk_fma_f16 v132, v130, v136, v132 op_sel:[1,1,0] op_sel_hi:[0,1,1] neg_lo:[0,1,0]\n"
        "v_pk_fma_f16 v133, v131, v136, v133 op_sel:[1,1,0] op_sel_hi:[0,1,1] neg_lo:[0,1,0]\n"
        "ds_write_addtid_b32 v132 offset:2880\n"
        "ds_write_addtid_b32 v133 offset:3168\n"
        "s_waitcnt lgkmcnt(12)\n"
        "v_mfma_f32_16x16x32_f16 v[186:189], v[168:171], v[152:155], v[186:189]\n"
        "v_pk_mul_f16 v134, v130, v137 op_sel_hi:[1,0]\n"
        "v_pk_mul_f16 v135, v131, v137 op_sel_hi:[1,0]\n"
        "v_pk_fma_f16 v134, v130, v137, v134 op_sel:[1,1,0] op_sel_hi:[0,1,1] neg_lo:[0,1,0]\n"
        "v_pk_fma_f16 v135, v131, v137, v135 op_sel:[1,1,0] op_sel_hi:[0,1,1] neg_lo:[0,1,0]\n"
        "ds_write_addtid_b32 v134 offset:3456\n"
        "ds_write_addtid_b32 v135 offset:3744\n"
        "v_pk_mul_f16 v132, v130, v138 op_sel_hi:[1,0]\n"
        "v_pk_mul_f16 v133, v131, v138 op_sel_hi:[1,0]\n"
        "v_pk_fma_f16 v132, v130, v138, v132 op_sel:[1,1,0] op_sel_hi:[0,1,1] neg_lo:[0,1,0]\n"
        "v_pk_fma_f16 v133, v131, v138, v133 op_sel:[1,1,0] op_sel_hi:[0,1,1] neg_lo:[0,1,0]\n"
        "ds_write_addtid_b32 v132 offset:4032\n"
        "ds_write_addtid_b32 v133 offset:4320\n"
        "ds_read_b128 v[62:65], v13 offset:0\n"
        "ds_read_b128 v[66:69], v13 offset:64\n"
        "ds_read_b128 v[70:73], v13 offset:128\n"
        "ds_read_b128 v[74:77], v13 offset:192\n"
        "v_cvt_pk_f16_f32 v126, v56, v57\n"
        "v_cvt_pk_f16_f32 v122, v118, v119\n"
        "v_cvt_pk_f16_f32 v124, v120, v121\n"
        "ds_write_addtid_b32 v122 offset:0\n"
        "ds_write_addtid_b32 v124 offset:1152\n"
        "ds_write_addtid_b32 v126 offset:2304\n"
        "v_pk_mul_f16 v134, v122, v136 op_sel_hi:[1,0]\n"
        "v_pk_mul_f16 v135, v124, v136 op_sel_hi:[1,0]\n"
        "v_pk_fma_f16 v134, v122, v136, v134 op_sel:[1,1,0] op_sel_hi:[0,1,1] neg_lo:[0,1,0]\n"
        "v_pk_fma_f16 v135, v124, v136, v135 op_sel:[1,1,0] op_sel_hi:[0,1,1] neg_lo:[0,1,0]\n"
        "ds_write_addtid_b32 v134 offset:288\n"
        "ds_write_addtid_b32 v135 offset:1440\n"
        "v_pk_mul_f16 v132, v122, v137 op_sel_hi:[1,0]\n"
        "v_pk_mul_f16 v133, v124, v137 op_sel_hi:[1,0]\n"
        "v_pk_fma_f16 v132, v122, v137, v132 op_sel:[1,1,0] op_sel_hi:[0,1,1] neg_lo:[0,1,0]\n"
        "v_pk_fma_f16 v133, v124, v137, v133 op_sel:[1,1,0] op_sel_hi:[0,1,1] neg_lo:[0,1,0]\n"
        "ds_write_addtid_b32 v132 offset:576\n"
        "ds_write_addtid_b32 v133 offset:1728\n"
        "v_pk_mul_f16 v134, v122, v138 op_sel_hi:[1,0]\n"
        "v_pk_mul_f16 v135, v124, v138 op_sel_hi:[1,0]\n"
        "v_pk_fma_f16 v134, v122, v138, v134 op_sel:[1,1,0] op_sel_hi:[0,1,1] neg_lo:[0,1,0]\n"
        "v_pk_fma_f16 v135, v124, v138, v135 op_sel:[1,1,0] op_sel_hi:[0,1,1] neg_lo:[0,1,0]\n"
        "ds_write_addtid_b32 v134 offset:864\n"
        "ds_write_addtid_b32 v135 offset:2016\n"
        "ds_read_b128 v[156:159], v13 offset:0\n"
        "ds_read_b128 v[160:163], v13 offset:64\n"
        "ds_read_b128 v[164:167], v13 offset:128\n"
        "ds_read_b128 v[168:171], v13 offset:192\n"
        "s_waitcnt lgkmcnt(14)\n"
        "v_mfma_f32_16x16x32_f16 v[190:193], v[62:65], v[140:143], 0\n"
        "v_mfma_f32_16x16x32_f16 v[190:193], v[66:69], v[144:147], v[190:193]\n"
        "v_mfma_f32_16x16x32_f16 v[190:193], v[70:73], v[148:151], v[190:193]\n"
        "s_waitcnt lgkmcnt(13)\n"
        "v_mfma_f32_16x16x32_f16 v[190:193], v[74:77], v[152:155], v[190:193]\n"
        "v_mov_b32 v46, s44\n"
        "v_mul_f32 v46, s41, v46\n"
        "v_add_f32_e64 v46, v46, v46\n"
        "ds_write_b128 v14, v[186:189]\n"
        "s_waitcnt lgkmcnt(4)\n"
        "v_mfma_f32_16x16x32_f16 v[194:197], v[156:159], v[140:143], 0\n"
        "s_waitcnt lgkmcnt(3)\n"
        "v_mfma_f32_16x16x32_f16 v[194:197], v[160:163], v[144:147], v[194:197]\n"
        "ds_write_b32 v15, v46\n"
        "s_waitcnt lgkmcnt(3)\n"
        "v_mfma_f32_16x16x32_f16 v[194:197], v[164:167], v[148:151], v[194:197]\n"
        "s_waitcnt lgkmcnt(2)\n"
        "v_mfma_f32_16x16x32_f16 v[194:197], v[168:171], v[152:155], v[194:197]\n"
        "ds_write_b128 v14, v[190:193] offset:1024\n"
        "s_add_u32 s46, s40, 1\n"
        "s_lshl_b32 s58, s46, 16\n"
        "s_mov_b32 s47, 32768\n"
        "s_mov_b32 s57, 0xffff\n"
        "s_add_u32 s50, s26, 1048576\n"
        "s_addc_u32 s51, s27, 0\n"
        "s_nop 0\n"
        "ds_write_b128 v14, v[194:197] offset:2048\n"
        "s_waitcnt lgkmcnt(0)\n"
        "s_barrier\n"
        "global_load_dwordx4 v[198:201], v11, s[28:29] offset:-4096 sc1\n"
        "global_load_dwordx4 v[202:205], v11, s[28:29] offset:0 sc1\n"
        "global_load_dwordx4 v[206:209], v12, s[28:29] offset:-4096 sc1\n"
        "global_load_dwordx4 v[210:213], v12, s[28:29] offset:0 sc1\n"
        "s_add_u32 s52, s26, 1572864\n"
        "s_addc_u32 s53, s27, 0\n"
        "ds_read_b128 v[154:157], v20\n"
        "s_waitcnt vmcnt(11)\n"
        "v_lshlrev_b32 v46, 16, v226\n"
        "s_waitcnt vmcnt(10)\n"
        "v_lshlrev_b32_e64 v47, 16, v228\n"
        "v_and_b32 v48, 0xffff0000, v226\n"
        "v_and_b32 v49, 0xffff0000, v228\n"
        "v_lshlrev_b32 v50, 16, v227\n"
        "v_lshlrev_b32 v51, 16, v229\n"
        "v_and_b32 v52, 0xffff0000, v227\n"
        "v_and_b32 v53, 0xffff0000, v229\n"
        "s_waitcnt vmcnt(9)\n"
        "v_lshlrev_b32 v54, 16, v230\n"
        "s_waitcnt vmcnt(8)\n"
        "v_lshlrev_b32 v55, 16, v232\n"
        "v_and_b32 v56, 0xffff0000, v230\n"
        "v_and_b32 v57, 0xffff0000, v232\n"
        "v_lshlrev_b32 v58, 16, v231\n"
        "v_lshlrev_b32 v59, 16, v233\n"
        "v_and_b32 v60, 0xffff0000, v231\n"
        "v_and_b32 v61, 0xffff0000, v233\n"
        "s_waitcnt vmcnt(7)\n"
        "v_lshlrev_b32 v62, 16, v234\n"
        "s_waitcnt vmcnt(6)\n"
        "v_lshlrev_b32 v63, 16, v236\n"
        "v_and_b32 v64, 0xffff0000, v234\n"
        "v_and_b32 v65, 0xffff0000, v236\n"
        "v_lshlrev_b32 v66, 16, v235\n"
        "v_lshlrev_b32 v67, 16, v237\n"
        "v_and_b32 v68, 0xffff0000, v235\n"
        "v_and_b32 v69, 0xffff0000, v237\n"
        "s_waitcnt vmcnt(5)\n"
        "v_lshlrev_b32 v70, 16, v238\n"
        "s_waitcnt vmcnt(4)\n"
        "v_lshlrev_b32 v71, 16, v240\n"
        "v_and_b32 v72, 0xffff0000, v238\n"
        "v_and_b32 v73, 0xffff0000, v240\n"
        "v_lshlrev_b32 v74, 16, v239\n"
        "v_lshlrev_b32 v75, 16, v241\n"
        "v_and_b32 v76, 0xffff0000, v239\n"
        "v_and_b32 v77, 0xffff0000, v241\n"
        "s_waitcnt lgkmcnt(0)\n"
        "v_rcp_f32 v146, v154\n"
        "v_rcp_f32 v147, v155\n"
        "v_rcp_f32 v148, v156\n"
        "v_rcp_f32_e64 v149, v157\n"
        "s_lshl_b32 s38, s42, 16\n"
        "s_and_b32 s39, s42, 0xffff0000\n"
        "s_lshl_b32 s54, s43, 16\n"
        "s_and_b32 s55, s43, 0xffff0000\n"
        "v_pk_mul_f32 v[150:151], v[146:147], s[38:39]\n"
        "v_pk_mul_f32 v[152:153], v[148:149], s[54:55]\n"
        "L_ECHK:\n"
        "s_waitcnt vmcnt(0)\n"
        "v_xor_b32 v130, s58, v199\n"
        "v_bitop3_b32 v130, v201, s58, v130 bitop3:0xbe\n"
        "v_bitop3_b32 v130, v203, s58, v130 bitop3:0xbe\n"
        "v_bitop3_b32 v130, v205, s58, v130 bitop3:0xbe\n"
        "v_bitop3_b32 v130, v207, s58, v130 bitop3:0xbe\n"
        "v_bitop3_b32 v130, v209, s58, v130 bitop3:0xbe\n"
        "v_bitop3_b32 v130, v211, s58, v130 bitop3:0xbe\n"
        "v_bitop3_b32 v130, v213, s58, v130 bitop3:0xbe\n"
        "v_cmp_lt_u32_e64 vcc, s57, v130\n"
        "s_sub_u32 s47, s47, 1\n"
        "s_cbranch_vccnz L_ERETRY\n"
        "L_EOK:\n"
        "v_and_b32 v130, 0xffff0000, v198\n"
        "v_and_b32 v131, 0xffff0000, v200\n"
        "v_rcp_f32 v130, v130\n"
        "v_rcp_f32 v131, v131\n"
        "v_lshlrev_b32 v132, 16, v198\n"
        "v_lshlrev_b32 v133, 16, v200\n"
        "v_pk_mul_f32 v[132:133], v[132:133], v[130:131] neg_lo:[1,0] neg_hi:[1,0]\n"
        "v_pk_fma_f32 v[78:79], v[46:47], v[130:131], v[132:133]\n"
        "v_pk_fma_f32 v[80:81], v[48:49], v[130:131], v[132:133]\n"
        "v_pk_fma_f32 v[82:83], v[50:51], v[130:131], v[132:133]\n"
        "v_pk_fma_f32 v[84:85], v[52:53], v[130:131], v[132:133]\n"
        "v_cvt_pk_f16_f32 v138, v78, v79\n"
        "v_cvt_pk_f16_f32 v139, v80, v81\n"
        "v_cvt_pk_f16_f32 v140, v82, v83\n"
        "v_cvt_pk_f16_f32 v141, v84, v85\n"
        "ds_write2st64_b32 v17, v138, v139 offset0:0 offset1:18\n"
        "ds_write2st64_b32 v17, v140, v141 offset0:36 offset1:54\n"
        "v_and_b32 v134, 0xffff0000, v202\n"
        "v_and_b32 v135, 0xffff0000, v204\n"
        "v_rcp_f32 v134, v134\n"
        "v_rcp_f32 v135, v135\n"
        "v_lshlrev_b32 v136, 16, v202\n"
        "v_lshlrev_b32 v137, 16, v204\n"
        "v_pk_mul_f32 v[136:137], v[136:137], v[134:135] neg_lo:[1,0] neg_hi:[1,0]\n"
        "v_pk_fma_f32 v[86:87], v[54:55], v[134:135], v[136:137]\n"
        "v_pk_fma_f32 v[88:89], v[56:57], v[134:135], v[136:137]\n"
        "v_pk_fma_f32 v[90:91], v[58:59], v[134:135], v[136:137]\n"
        "v_pk_fma_f32 v[92:93], v[60:61], v[134:135], v[136:137]\n"
        "v_cvt_pk_f16_f32 v138, v86, v87\n"
        "v_cvt_pk_f16_f32 v139, v88, v89\n"
        "v_cvt_pk_f16_f32 v140, v90, v91\n"
        "v_cvt_pk_f16_f32 v141, v92, v93\n"
        "ds_write2st64_b32 v17, v138, v139 offset0:4 offset1:22\n"
        "ds_write2st64_b32 v17, v140, v141 offset0:40 offset1:58\n"
        "v_and_b32 v130, 0xffff0000, v206\n"
        "v_and_b32 v131, 0xffff0000, v208\n"
        "v_rcp_f32 v130, v130\n"
        "v_rcp_f32 v131, v131\n"
        "v_lshlrev_b32 v132, 16, v206\n"
        "v_lshlrev_b32 v133, 16, v208\n"
        "v_pk_mul_f32 v[132:133], v[132:133], v[130:131] neg_lo:[1,0] neg_hi:[1,0]\n"
        "v_pk_fma_f32 v[94:95], v[62:63], v[130:131], v[132:133]\n"
        "v_pk_fma_f32 v[96:97], v[64:65], v[130:131], v[132:133]\n"
        "v_pk_fma_f32 v[98:99], v[66:67], v[130:131], v[132:133]\n"
        "v_pk_fma_f32 v[100:101], v[68:69], v[130:131], v[132:133]\n"
        "v_cvt_pk_f16_f32 v138, v94, v95\n"
        "v_cvt_pk_f16_f32 v139, v96, v97\n"
        "v_cvt_pk_f16_f32 v140, v98, v99\n"
        "v_cvt_pk_f16_f32 v141, v100, v101\n"
        "ds_write2st64_b32 v17, v138, v139 offset0:8 offset1:26\n"
        "ds_write2st64_b32 v17, v140, v141 offset0:44 offset1:62\n"
        "v_and_b32 v134, 0xffff0000, v210\n"
        "v_and_b32 v135, 0xffff0000, v212\n"
        "v_rcp_f32 v134, v134\n"
        "v_rcp_f32 v135, v135\n"
        "v_lshlrev_b32 v136, 16, v210\n"
        "v_lshlrev_b32 v137, 16, v212\n"
        "v_pk_mul_f32 v[136:137], v[136:137], v[134:135] neg_lo:[1,0] neg_hi:[1,0]\n"
        "v_pk_fma_f32 v[102:103], v[70:71], v[134:135], v[136:137]\n"
        "v_pk_fma_f32 v[104:105], v[72:73], v[134:135], v[136:137]\n"
        "v_pk_fma_f32 v[106:107], v[74:75], v[134:135], v[136:137]\n"
        "v_pk_fma_f32 v[108:109], v[76:77], v[134:135], v[136:137]\n"
        "v_cvt_pk_f16_f32 v138, v102, v103\n"
        "v_cvt_pk_f16_f32 v139, v104, v105\n"
        "v_cvt_pk_f16_f32 v140, v106, v107\n"
        "v_cvt_pk_f16_f32 v141, v108, v109\n"
        "ds_write2st64_b32 v17, v138, v139 offset0:12 offset1:30\n"
        "ds_write2st64_b32 v17, v140, v141 offset0:48 offset1:66\n"
        "s_mov_b32 s54, 0xbfb8aa3b\n"
        "s_waitcnt lgkmcnt(0)\n"
        "s_barrier\n"
        "s_add_u32 s48, s26, 524288\n"
        "s_addc_u32 s49, s27, 0\n"
        "s_cmp_lg_u32 s30, 4\n"
        "s_cbranch_scc1 L_ENOAL\n"
        "s_lshl_b32 s38, s33, 7\n"
        "s_add_u32 s38, s38, 32768\n"
        "v_mov_b32 v4, s38\n"
        "v_mov_b32 v130, s46\n"
        "v_not_b32 v131, v130\n"
        "s_mov_b64 exec, 1\n"
        "global_store_dwordx2 v4, v[130:131], s[28:29] sc1\n"
        "s_mov_b64 exec, -1\n"
        "L_ENOAL:\n"
        "v_mul_f32 v110, s54, v78\n"
        "v_mul_f32 v111, s54, v79\n"
        "v_mul_f32 v112, s54, v80\n"
        "v_mul_f32 v113, s54, v81\n"
        "v_mul_f32 v114, s54, v82\n"
        "v_mul_f32 v115, s54, v83\n"
        "v_mul_f32 v116, s54, v84\n"
        "v_mul_f32 v117, s54, v85\n"
        "v_exp_f32 v110, v110\n"
        "v_exp_f32 v111, v111\n"
        "v_exp_f32 v112, v112\n"
        "v_exp_f32 v113, v113\n"
        "v_exp_f32 v114, v114\n"
        "v_exp_f32 v115, v115\n"
        "v_exp_f32 v116, v116\n"
        "v_exp_f32_e64 v117, v117\n"
        "v_fma_f32 v110, v110, v146, v146\n"
        "v_fma_f32 v111, v111, v146, v146\n"
        "v_fma_f32 v112, v112, v147, v147\n"
        "v_fma_f32 v113, v113, v147, v147\n"
        "v_fma_f32 v114, v114, v148, v148\n"
        "v_fma_f32 v115, v115, v148, v148\n"
        "v_fma_f32 v116, v116, v149, v149\n"
        "v_fma_f32 v117, v117, v149, v149\n"
        "v_rcp_f32 v110, v110\n"
        "v_rcp_f32 v111, v111\n"
        "v_rcp_f32 v112, v112\n"
        "v_rcp_f32 v113, v113\n"
        "v_rcp_f32 v114, v114\n"
        "v_rcp_f32 v115, v115\n"
        "v_rcp_f32 v116, v116\n"
        "v_rcp_f32 v117, v117\n"
        "s_mov_b32 s47, 32768\n"
        "ds_read_b32 v126, v19 offset:0\n"
        "ds_read_b64 v[118:119], v18 offset:0\n"
        "ds_read_b64 v[120:121], v18 offset:10240\n"
        "ds_read_b64 v[122:123], v18 offset:20480\n"
        "ds_read_b64 v[124:125], v18 offset:30720\n"
        "v_mul_f32 v110, v110, v78\n"
        "v_mul_f32 v111, v111, v79\n"
        "v_mul_f32 v112, v112, v80\n"
        "v_mul_f32 v113, v113, v81\n"
        "v_mul_f32 v114, v114, v82\n"
        "v_mul_f32 v115, v115, v83\n"
        "v_mul_f32 v116, v116, v84\n"
        "v_mul_f32 v117, v117, v85\n"
        "L_EW0:\n"
        "s_waitcnt lgkmcnt(4)\n"
        "v_cmp_gt_u32_e64 vcc, 4, v126\n"
        "s_sub_u32 s47, s47, 1\n"
        "s_cbranch_vccz L_EG0\n"
        "s_cbranch_scc1 L_EG0\n"
        "s_waitcnt lgkmcnt(0)\n"
        "s_sleep 1\n"
        "ds_read_b32 v126, v19 offset:0\n"
        "ds_read_b64 v[118:119], v18 offset:0\n"
        "ds_read_b64 v[120:121], v18 offset:10240\n"
        "ds_read_b64 v[122:123], v18 offset:20480\n"
        "ds_read_b64 v[124:125], v18 offset:30720\n"
        "s_branch L_EW0\n"
        "L_EG0:\n"
        "s_waitcnt lgkmcnt(3)\n"
        "v_fmac_f32 v118, v78, v150\n"
        "v_fmac_f32 v119, v79, v150\n"
        "s_waitcnt lgkmcnt(2)\n"
        "v_fmac_f32 v120, v80, v151\n"
        "v_fmac_f32 v121, v81, v151\n"
        "s_waitcnt lgkmcnt(1)\n"
        "v_fmac_f32 v122, v82, v152\n"
        "v_fmac_f32 v123, v83, v152\n"
        "s_waitcnt lgkmcnt(0)\n"
        "v_fmac_f32 v124, v84, v153\n"
        "v_fmac_f32_e64 v125, v85, v153\n"
        "v_fma_f32 v118, v118, v110, v46\n"
        "v_fma_f32 v119, v119, v111, v47\n"
        "v_fma_f32 v120, v120, v112, v48\n"
        "v_fma_f32 v121, v121, v113, v49\n"
        "v_fma_f32 v122, v122, v114, v50\n"
        "v_fma_f32 v123, v123, v115, v51\n"
        "v_fma_f32 v124, v124, v116, v52\n"
        "v_fma_f32 v125, v125, v117, v53\n"
        "v_cvt_pk_bf16_f32 v130, v118, v120\n"
        "v_cvt_pk_bf16_f32 v131, v122, v124\n"
        "global_store_dwordx2 v10, v[130:131], s[26:27] offset:0\n"
        "v_cvt_pk_bf16_f32 v132, v119, v121\n"
        "v_cvt_pk_bf16_f32 v133, v123, v125\n"
        "global_store_dwordx2 v10, v[132:133], s[26:27] offset:1024\n"
        "v_mul_f32 v110, s54, v86\n"
        "v_mul_f32 v111, s54, v87\n"
        "v_mul_f32 v112, s54, v88\n"
        "v_mul_f32 v113, s54, v89\n"
        "v_mul_f32 v114, s54, v90\n"
        "v_mul_f32 v115, s54, v91\n"
        "v_mul_f32 v116, s54, v92\n"
        "v_mul_f32 v117, s54, v93\n"
        "v_exp_f32 v110, v110\n"
        "v_exp_f32 v111, v111\n"
        "v_exp_f32 v112, v112\n"
        "v_exp_f32 v113, v113\n"
        "v_exp_f32 v114, v114\n"
        "v_exp_f32 v115, v115\n"
        "v_exp_f32 v116, v116\n"
        "v_exp_f32 v117, v117\n"
        "v_fma_f32 v110, v110, v146, v146\n"
        "v_fma_f32 v111, v111, v146, v146\n"
        "v_fma_f32 v112, v112, v147, v147\n"
        "v_fma_f32 v113, v113, v147, v147\n"
        "v_fma_f32 v114, v114, v148, v148\n"
        "v_fma_f32 v115, v115, v148, v148\n"
        "v_fma_f32 v116, v116, v149, v149\n"
        "v_fma_f32 v117, v117, v149, v149\n"
        "v_rcp_f32 v110, v110\n"
        "v_rcp_f32 v111, v111\n"
        "v_rcp_f32 v112, v112\n"
        "v_rcp_f32 v113, v113\n"
        "v_rcp_f32 v114, v114\n"
        "v_rcp_f32 v115, v115\n"
        "v_rcp_f32 v116, v116\n"
        "v_rcp_f32 v117, v117\n"
        "s_mov_b32 s47, 32768\n"
        "ds_read_b32 v126, v19 offset:512\n"
        "ds_read_b64 v[118:119], v18 offset:2560\n"
        "ds_read_b64 v[120:121], v18 offset:12800\n"
        "ds_read_b64 v[122:123], v18 offset:23040\n"
        "ds_read_b64 v[124:125], v18 offset:33280\n"
        "v_mul_f32 v110, v110, v86\n"
        "v_mul_f32 v111, v111, v87\n"
        "v_mul_f32 v112, v112, v88\n"
        "v_mul_f32 v113, v113, v89\n"
        "v_mul_f32 v114, v114, v90\n"
        "v_mul_f32 v115, v115, v91\n"
        "v_mul_f32 v116, v116, v92\n"
        "v_mul_f32 v117, v117, v93\n"
        "L_EW1:\n"
        "s_waitcnt lgkmcnt(4)\n"
        "v_cmp_gt_u32_e64 vcc, 4, v126\n"
        "s_sub_u32 s47, s47, 1\n"
        "s_cbranch_vccz L_EG1\n"
        "s_cbranch_scc1 L_EG1\n"
        "s_waitcnt lgkmcnt(0)\n"
        "s_sleep 1\n"
        "ds_read_b32 v126, v19 offset:512\n"
        "ds_read_b64 v[118:119], v18 offset:2560\n"
        "ds_read_b64 v[120:121], v18 offset:12800\n"
        "ds_read_b64 v[122:123], v18 offset:23040\n"
        "ds_read_b64 v[124:125], v18 offset:33280\n"
        "s_branch L_EW1\n"
        "L_EG1:\n"
        "s_waitcnt lgkmcnt(3)\n"
        "v_fmac_f32 v118, v86, v150\n"
        "v_fmac_f32 v119, v87, v150\n"
        "s_waitcnt lgkmcnt(2)\n"
        "v_fmac_f32 v120, v88, v151\n"
        "v_fmac_f32 v121, v89, v151\n"
        "s_waitcnt lgkmcnt(1)\n"
        "v_fmac_f32 v122, v90, v152\n"
        "v_fmac_f32 v123, v91, v152\n"
        "s_waitcnt lgkmcnt(0)\n"
        "v_fmac_f32 v124, v92, v153\n"
        "v_fmac_f32_e64 v125, v93, v153\n"
        "v_fma_f32 v118, v118, v110, v54\n"
        "v_fma_f32 v119, v119, v111, v55\n"
        "v_fma_f32 v120, v120, v112, v56\n"
        "v_fma_f32 v121, v121, v113, v57\n"
        "v_fma_f32 v122, v122, v114, v58\n"
        "v_fma_f32 v123, v123, v115, v59\n"
        "v_fma_f32 v124, v124, v116, v60\n"
        "v_fma_f32 v125, v125, v117, v61\n"
        "v_cvt_pk_bf16_f32 v134, v118, v120\n"
        "v_cvt_pk_bf16_f32 v135, v122, v124\n"
        "global_store_dwordx2 v10, v[134:135], s[48:49] offset:0\n"
        "v_cvt_pk_bf16_f32 v136, v119, v121\n"
        "v_cvt_pk_bf16_f32 v137, v123, v125\n"
        "global_store_dwordx2 v10, v[136:137], s[48:49] offset:1024\n"
        "v_mul_f32 v110, s54, v94\n"
        "v_mul_f32 v111, s54, v95\n"
        "v_mul_f32 v112, s54, v96\n"
        "v_mul_f32 v113, s54, v97\n"
        "v_mul_f32 v114, s54, v98\n"
        "v_mul_f32 v115, s54, v99\n"
        "v_mul_f32 v116, s54, v100\n"
        "v_mul_f32 v117, s54, v101\n"
        "v_exp_f32 v110, v110\n"
        "v_exp_f32 v111, v111\n"
        "v_exp_f32 v112, v112\n"
        "v_exp_f32 v113, v113\n"
        "v_exp_f32 v114, v114\n"
        "v_exp_f32 v115, v115\n"
        "v_exp_f32 v116, v116\n"
        "v_exp_f32 v117, v117\n"
        "v_fma_f32 v110, v110, v146, v146\n"
        "v_fma_f32 v111, v111, v146, v146\n"
        "v_fma_f32 v112, v112, v147, v147\n"
        "v_fma_f32 v113, v113, v147, v147\n"
        "v_fma_f32 v114, v114, v148, v148\n"
        "v_fma_f32 v115, v115, v148, v148\n"
        "v_fma_f32 v116, v116, v149, v149\n"
        "v_fma_f32 v117, v117, v149, v149\n"
        "v_rcp_f32 v110, v110\n"
        "v_rcp_f32 v111, v111\n"
        "v_rcp_f32 v112, v112\n"
        "v_rcp_f32 v113, v113\n"
        "v_rcp_f32 v114, v114\n"
        "v_rcp_f32 v115, v115\n"
        "v_rcp_f32 v116, v116\n"
        "v_rcp_f32 v117, v117\n"
        "s_mov_b32 s47, 32768\n"
        "ds_read_b32 v126, v19 offset:1024\n"
        "ds_read_b64 v[118:119], v18 offset:5120\n"
        "ds_read_b64 v[120:121], v18 offset:15360\n"
        "ds_read_b64 v[122:123], v18 offset:25600\n"
        "ds_read_b64 v[124:125], v18 offset:35840\n"
        "v_mul_f32 v110, v110, v94\n"
        "v_mul_f32 v111, v111, v95\n"
        "v_mul_f32 v112, v112, v96\n"
        "v_mul_f32 v113, v113, v97\n"
        "v_mul_f32 v114, v114, v98\n"
        "v_mul_f32 v115, v115, v99\n"
        "v_mul_f32 v116, v116, v100\n"
        "v_mul_f32 v117, v117, v101\n"
        "L_EW2:\n"
        "s_waitcnt lgkmcnt(4)\n"
        "v_cmp_gt_u32_e64 vcc, 4, v126\n"
        "s_sub_u32 s47, s47, 1\n"
        "s_cbranch_vccz L_EG2\n"
        "s_cbranch_scc1 L_EG2\n"
        "s_waitcnt lgkmcnt(0)\n"
        "s_sleep 1\n"
        "ds_read_b32 v126, v19 offset:1024\n"
        "ds_read_b64 v[118:119], v18 offset:5120\n"
        "ds_read_b64 v[120:121], v18 offset:15360\n"
        "ds_read_b64 v[122:123], v18 offset:25600\n"
        "ds_read_b64 v[124:125], v18 offset:35840\n"
        "s_branch L_EW2\n"
        "L_EG2:\n"
        "s_waitcnt lgkmcnt(3)\n"
        "v_fmac_f32 v118, v94, v150\n"
        "v_fmac_f32 v119, v95, v150\n"
        "s_waitcnt lgkmcnt(2)\n"
        "v_fmac_f32 v120, v96, v151\n"
        "v_fmac_f32 v121, v97, v151\n"
        "s_waitcnt lgkmcnt(1)\n"
        "v_fmac_f32 v122, v98, v152\n"
        "v_fmac_f32 v123, v99, v152\n"
        "s_waitcnt lgkmcnt(0)\n"
        "v_fmac_f32 v124, v100, v153\n"
        "v_fmac_f32_e64 v125, v101, v153\n"
        "v_fma_f32 v118, v118, v110, v62\n"
        "v_fma_f32 v119, v119, v111, v63\n"
        "v_fma_f32 v120, v120, v112, v64\n"
        "v_fma_f32 v121, v121, v113, v65\n"
        "v_fma_f32 v122, v122, v114, v66\n"
        "v_fma_f32 v123, v123, v115, v67\n"
        "v_fma_f32 v124, v124, v116, v68\n"
        "v_fma_f32 v125, v125, v117, v69\n"
        "v_cvt_pk_bf16_f32 v130, v118, v120\n"
        "v_cvt_pk_bf16_f32 v131, v122, v124\n"
        "global_store_dwordx2 v10, v[130:131], s[50:51] offset:0\n"
        "v_cvt_pk_bf16_f32 v132, v119, v121\n"
        "v_cvt_pk_bf16_f32 v133, v123, v125\n"
        "global_store_dwordx2 v10, v[132:133], s[50:51] offset:1024\n"
        "v_mul_f32 v110, s54, v102\n"
        "v_mul_f32 v111, s54, v103\n"
        "v_mul_f32 v112, s54, v104\n"
        "v_mul_f32 v113, s54, v105\n"
        "v_mul_f32 v114, s54, v106\n"
        "v_mul_f32 v115, s54, v107\n"
        "v_mul_f32 v116, s54, v108\n"
        "v_mul_f32 v117, s54, v109\n"
        "v_exp_f32 v110, v110\n"
        "v_exp_f32 v111, v111\n"
        "v_exp_f32 v112, v112\n"
        "v_exp_f32 v113, v113\n"
        "v_exp_f32 v114, v114\n"
        "v_exp_f32 v115, v115\n"
        "v_exp_f32 v116, v116\n"
        "v_exp_f32 v117, v117\n"
        "v_fma_f32 v110, v110, v146, v146\n"
        "v_fma_f32 v111, v111, v146, v146\n"
        "v_fma_f32 v112, v112, v147, v147\n"
        "v_fma_f32 v113, v113, v147, v147\n"
        "v_fma_f32 v114, v114, v148, v148\n"
        "v_fma_f32 v115, v115, v148, v148\n"
        "v_fma_f32 v116, v116, v149, v149\n"
        "v_fma_f32 v117, v117, v149, v149\n"
        "v_rcp_f32 v110, v110\n"
        "v_rcp_f32 v111, v111\n"
        "v_rcp_f32 v112, v112\n"
        "v_rcp_f32 v113, v113\n"
        "v_rcp_f32 v114, v114\n"
        "v_rcp_f32 v115, v115\n"
        "v_rcp_f32 v116, v116\n"
        "v_rcp_f32 v117, v117\n"
        "s_mov_b32 s47, 32768\n"
        "ds_read_b32 v126, v19 offset:1536\n"
        "ds_read_b64 v[118:119], v18 offset:7680\n"
        "ds_read_b64 v[120:121], v18 offset:17920\n"
        "ds_read_b64 v[122:123], v18 offset:28160\n"
        "ds_read_b64 v[124:125], v18 offset:38400\n"
        "v_mul_f32 v110, v110, v102\n"
        "v_mul_f32 v111, v111, v103\n"
        "v_mul_f32 v112, v112, v104\n"
        "v_mul_f32 v113, v113, v105\n"
        "v_mul_f32 v114, v114, v106\n"
        "v_mul_f32 v115, v115, v107\n"
        "v_mul_f32 v116, v116, v108\n"
        "v_mul_f32 v117, v117, v109\n"
        "L_EW3:\n"
        "s_waitcnt lgkmcnt(4)\n"
        "v_cmp_gt_u32_e64 vcc, 4, v126\n"
        "s_sub_u32 s47, s47, 1\n"
        "s_cbranch_vccz L_EG3\n"
        "s_cbranch_scc1 L_EG3\n"
        "s_waitcnt lgkmcnt(0)\n"
        "s_sleep 1\n"
        "ds_read_b32 v126, v19 offset:1536\n"
        "ds_read_b64 v[118:119], v18 offset:7680\n"
        "ds_read_b64 v[120:121], v18 offset:17920\n"
        "ds_read_b64 v[122:123], v18 offset:28160\n"
        "ds_read_b64 v[124:125], v18 offset:38400\n"
        "s_branch L_EW3\n"
        "L_EG3:\n"
        "s_waitcnt lgkmcnt(3)\n"
        "v_fmac_f32 v118, v102, v150\n"
        "v_fmac_f32 v119, v103, v150\n"
        "s_waitcnt lgkmcnt(2)\n"
        "v_fmac_f32 v120, v104, v151\n"
        "v_fmac_f32 v121, v105, v151\n"
        "s_waitcnt lgkmcnt(1)\n"
        "v_fmac_f32 v122, v106, v152\n"
        "v_fmac_f32_e64 v123, v107, v152\n"
        "s_waitcnt lgkmcnt(0)\n"
        "v_fmac_f32_e64 v124, v108, v153\n"
        "v_fmac_f32_e64 v125, v109, v153\n"
        "v_fma_f32 v118, v118, v110, v70\n"
        "v_fma_f32 v119, v119, v111, v71\n"
        "v_fma_f32 v120, v120, v112, v72\n"
        "v_fma_f32 v121, v121, v113, v73\n"
        "v_fma_f32 v122, v122, v114, v74\n"
        "v_fma_f32 v123, v123, v115, v75\n"
        "v_fma_f32 v124, v124, v116, v76\n"
        "v_fma_f32 v125, v125, v117, v77\n"
        "v_cvt_pk_bf16_f32 v134, v118, v120\n"
        "v_cvt_pk_bf16_f32 v135, v122, v124\n"
        "global_store_dwordx2 v10, v[134:135], s[52:53] offset:0\n"
        "v_cvt_pk_bf16_f32 v136, v119, v121\n"
        "v_cvt_pk_bf16_f32 v137, v123, v125\n"
        "global_store_dwordx2 v10, v[136:137], s[52:53] offset:1024\n"
        "L_ESKIP:\n"
        "s_barrier\n"
        "s_endpgm\n"
        "L_ERETRY:\n"
        "s_cbranch_scc1 L_EOK\n"
        "global_load_dwordx4 v[198:201], v11, s[28:29] offset:-4096 sc1\n"
        "global_load_dwordx4 v[202:205], v11, s[28:29] offset:0 sc1\n"
        "global_load_dwordx4 v[206:209], v12, s[28:29] offset:-4096 sc1\n"
        "global_load_dwordx4 v[210:213], v12, s[28:29] offset:0 sc1\n"
        "s_branch L_ECHK\n"
        ".fill 6, 4, 0xbf800000\n"
        "L_AUX:\n"
        "v_lshlrev_b32 v8, 4, v1\n"
        "s_lshl3_add_u32 s35, s33, s30\n"
        "s_lshl_b32 s39, s35, 11\n"
        "v_add_u32_e64 v9, s39, v8\n"
        "s_waitcnt lgkmcnt(0)\n"
        "global_load_dwordx4 v[226:229], v9, s[4:5]\n"
        "global_load_dwordx4 v[230:233], v9, s[4:5] offset:1024\n"
        "global_load_dwordx4 v[234:237], v8, s[18:19]\n"
        "s_load_dword s40, s[28:29], 0x0\n"
        "s_mul_i32 s60, s31, 43\n"
        "s_add_u32 s61, s60, 43\n"
        "s_lshr_b32 s60, s60, 3\n"
        "s_lshr_b32 s61, s61, 3\n"
        "s_cmp_ge_u32 s32, 10\n"
        "s_cbranch_scc1 L_AF\n"
        "s_and_b32 s38, s35, 127\n"
        "s_cmp_ge_u32 s32, 8\n"
        "s_cbranch_scc1 L_AHX\n"
        "s_sub_u32 s55, s61, s60\n"
        "s_lshl_b32 s55, s55, 6\n"
        "s_sub_u32 s55, s55, 4\n"
        "s_and_b32 s39, s38, 32\n"
        "s_add_u32 s39, s39, s38\n"
        "s_add_u32 s39, s39, 32\n"
        "s_mul_i32 s39, s39, 11008\n"
        "s_lshl_b32 s54, s60, 6\n"
        "s_add_u32 s39, s39, s54\n"
        "v_and_b32 v40, 3, v1\n"
        "v_lshlrev_b32 v40, 7, v40\n"
        "v_min_u32_e64 v40, s55, v40\n"
        "v_bfe_u32 v5, v1, 2, 2\n"
        "v_mul_u32_u24 v5, 2752, v5\n"
        "v_add3_u32 v40, v40, v5, s39\n"
        "v_mov_b32 v41, v40\n"
        "s_mov_b64 s[48:49], s[20:21]\n"
        "s_mov_b64 s[50:51], s[22:23]\n"
        "s_mul_i32 s38, s38, 24\n"
        "s_lshl_b32 s54, s60, 8\n"
        "s_add_u32 s38, s38, s54\n"
        "v_add_u32_e64 v42, s38, v1\n"
        "v_min_u32 v42, 11007, v42\n"
        "v_lshlrev_b32_e64 v42, 7, v42\n"
        "s_mov_b32 s62, 0xffffff\n"
        "s_branch L_AH1\n"
        "L_AHX:\n"
        "s_sub_u32 s38, s38, 64\n"
        "s_mul_i32 s38, s38, 16384\n"
        "s_lshl_b32 s39, s31, 7\n"
        "s_add_u32 s38, s38, s39\n"
        "s_add_u32 s38, s38, 1572864\n"
        "v_min_u32 v40, 15, v1\n"
        "v_lshl_add_u32 v40, v40, 10, s38\n"
        "v_min_u32 v40, 2097024, v40\n"
        "v_add_u32 v41, 262144, v40\n"
        "v_min_u32 v41, 2097024, v41\n"
        "s_mov_b64 s[48:49], s[4:5]\n"
        "s_mov_b64 s[50:51], s[4:5]\n"
        "s_mov_b32 s62, 0\n"
        "L_AH1:\n"
        "s_mov_b64 exec, 0xffff\n"
        "s_barrier\n"
        "global_load_dword v250, v40, s[48:49]\n"
        "s_mov_b64 exec, -1\n"
        "s_waitcnt vmcnt(1)\n"
        "s_branch L_ALN\n"
        "L_AF:\n"
        "s_add_u32 s54, s60, s32\n"
        "s_sub_u32 s54, s54, 10\n"
        "s_cmp_ge_u32 s54, s61\n"
        "s_cselect_b32 s54, 99, s54\n"
        "s_lshl_b32 s38, s30, 2\n"
        "s_lshr_b32 s38, 0x76325410, s38\n"
        "s_lshl2_add_u32 s55, s54, s38\n"
        "s_and_b32 s55, s55, 7\n"
        "s_mul_i32 s38, s55, 176128\n"
        "s_lshl_b32 s39, s54, 6\n"
        "s_add_u32 s38, s38, s39\n"
        "v_lshrrev_b32_e64 v3, 2, v1\n"
        "v_mul_u32_u24 v3, 2752, v3\n"
        "v_and_b32_e64 v5, 32, v1\n"
        "v_xor_b32_e64 v5, v5, v8\n"
        "v_and_b32_e64 v5, 48, v5\n"
        "v_add3_u32 v44, v3, v5, s38\n"
        "v_add_u32 v45, 44032, v44\n"
        "v_add_u32 v46, 88064, v44\n"
        "v_add_u32 v47, 132096, v44\n"
        "s_mul_i32 s63, s30, 12288\n"
        "s_add_u32 s63, s63, 27136\n"
        "s_lshl_b32 s38, s30, 8\n"
        "s_add_u32 s38, s38, s54\n"
        "s_cmp_lt_u32 s38, 43\n"
        "s_barrier\n"
        "s_cbranch_scc0 L_AW0\n"
        "s_add_u32 m0, s63, 0\n"
        "s_nop 0\n"
        "global_load_lds_dwordx4 v44, s[20:21]\n"
        "s_add_u32 m0, s63, 4096\n"
        "s_nop 0\n"
        "global_load_lds_dwordx4 v44, s[22:23]\n"
        "s_add_u32 m0, s63, 1024\n"
        "s_nop 0\n"
        "global_load_lds_dwordx4 v45, s[20:21]\n"
        "s_add_u32 m0, s63, 5120\n"
        "s_nop 0\n"
        "global_load_lds_dwordx4 v45, s[22:23]\n"
        "s_add_u32 m0, s63, 2048\n"
        "s_nop 0\n"
        "global_load_lds_dwordx4 v46, s[20:21]\n"
        "s_nop 0\n"
        "s_add_u32 m0, s63, 6144\n"
        "s_nop 0\n"
        "global_load_lds_dwordx4 v46, s[22:23]\n"
        "s_add_u32 m0, s63, 3072\n"
        "s_nop 0\n"
        "global_load_lds_dwordx4 v47, s[20:21]\n"
        "s_add_u32 m0, s63, 7168\n"
        "s_nop 0\n"
        "global_load_lds_dwordx4 v47, s[22:23]\n"
        "s_waitcnt vmcnt(8)\n"
        "s_branch L_ALN\n"
        "L_AW0:\n"
        "s_waitcnt vmcnt(0)\n"
        "L_ALN:\n"
        "v_mov_b32 v4, 0x3f803f80\n"
        "v_dot2_f32_bf16 v12, v226, v226, 0\n"
        "v_dot2_f32_bf16 v13, v230, v230, 0\n"
        "v_dot2_f32_bf16 v14, v226, v234, 0\n"
        "v_dot2_f32_bf16 v15, v230, v234, 0\n"
        "v_dot2_f32_bf16 v10, v226, v4, 0\n"
        "v_dot2_f32_bf16 v11, v230, v4, 0\n"
        "v_dot2_f32_bf16 v16, v234, v4, 0\n"
        "v_dot2_f32_bf16 v12, v227, v227, v12\n"
        "v_dot2_f32_bf16 v13, v231, v231, v13\n"
        "v_dot2_f32_bf16 v14, v227, v235, v14\n"
        "v_dot2_f32_bf16 v15, v231, v235, v15\n"
        "v_dot2_f32_bf16 v10, v227, v4, v10\n"
        "v_dot2_f32_bf16 v11, v231, v4, v11\n"
        "v_dot2_f32_bf16 v16, v235, v4, v16\n"
        "v_dot2_f32_bf16 v12, v228, v228, v12\n"
        "v_dot2_f32_bf16 v13, v232, v232, v13\n"
        "v_dot2_f32_bf16 v14, v228, v236, v14\n"
        "v_dot2_f32_bf16 v15, v232, v236, v15\n"
        "v_dot2_f32_bf16 v10, v228, v4, v10\n"
        "v_dot2_f32_bf16 v11, v232, v4, v11\n"
        "v_dot2_f32_bf16 v16, v236, v4, v16\n"
        "v_dot2_f32_bf16 v12, v229, v229, v12\n"
        "v_dot2_f32_bf16 v13, v233, v233, v13\n"
        "v_dot2_f32_bf16 v14, v229, v237, v14\n"
        "v_dot2_f32_bf16 v15, v233, v237, v15\n"
        "v_dot2_f32_bf16 v10, v229, v4, v10\n"
        "v_dot2_f32_bf16 v11, v233, v4, v11\n"
        "v_dot2_f32_bf16 v16, v237, v4, v16\n"
        "s_waitcnt lgkmcnt(0)\n"
        "v_permlane32_swap_b32 v12, v13\n"
        "v_permlane32_swap_b32 v14, v15\n"
        "v_permlane32_swap_b32 v10, v11\n"
        "v_mov_b32 v17, v16\n"
        "v_add_f32 v12, v12, v13\n"
        "v_add_f32 v10, v10, v11\n"
        "v_permlane32_swap_b32 v16, v17\n"
        "v_add_f32 v14, v14, v15\n"
        "v_permlane16_swap_b32 v10, v12\n"
        "v_add_f32 v16, v16, v17\n"
        "s_add_u32 s46, s40, 1\n"
        "v_add_f32_e64 v10, v10, v12\n"
        "v_permlane16_swap_b32 v14, v16\n"
        "s_lshl_b32 s45, s46, 16\n"
        "v_add_f32_dpp v10, v10, v10 row_ror:8 row_mask:0xf bank_mask:0xf\n"
        "v_add_f32_e64 v14, v14, v16\n"
        "v_mov_b32 v24, s45\n"
        "v_add_f32_dpp v10, v10, v10 row_ror:4 row_mask:0xf bank_mask:0xf\n"
        "v_add_f32_dpp v14, v14, v14 row_ror:8 row_mask:0xf bank_mask:0xf\n"
        "s_lshl_b32 s38, s35, 4\n"
        "v_add_f32_dpp v10, v10, v10 row_ror:2 row_mask:0xf bank_mask:0xf\n"
        "v_add_f32_dpp v14, v14, v14 row_ror:4 row_mask:0xf bank_mask:0xf\n"
        "s_add_u32 s38, s38, 4096\n"
        "v_add_f32_dpp v10, v10, v10 row_ror:1 row_mask:0xf bank_mask:0xf\n"
        "v_add_f32_dpp v14, v14, v14 row_ror:2 row_mask:0xf bank_mask:0xf\n"
        "v_mul_f32 v10, 0x3b000000, v10\n"
        "v_mov_b32 v12, v10\n"
        "v_add_f32_dpp v14, v14, v14 row_ror:1 row_mask:0xf bank_mask:0xf\n"
        "v_mov_b32_e64 v3, s38\n"
        "v_permlane16_swap_b32 v10, v12\n"
        "v_mov_b32 v16, v14\n"
        "v_fma_f32 v12, -v10, v10, v12\n"
        "v_max_f32_e64 v12, 0, v12\n"
        "v_permlane16_swap_b32 v14, v16\n"
        "v_cvt_pk_bf16_f32 v18, v10, v12\n"
        "v_and_b32 v19, 0xffff0000, v18\n"
        "v_add_f32 v19, 0x3727c5ac, v19\n"
        "v_cvt_pk_bf16_f32 v19, v19, v19\n"
        "v_lshlrev_b32_e64 v19, 16, v19\n"
        "v_sqrt_f32_e64 v19, v19\n"
        "v_lshlrev_b32_e64 v18, 16, v18\n"
        "v_cvt_pk_bf16_f32 v34, v10, v19\n"
        "v_and_b32 v19, 0xffff0000, v34\n"
        "v_rcp_f32_e64 v19, v19\n"
        "v_fma_f32 v14, -v18, v16, v14\n"
        "v_mov_b32_e64 v36, v34\n"
        "v_mul_f32_e64 v14, v14, v19\n"
        "s_mov_b32 s58, 0xffff\n"
        "v_cvt_pk_bf16_f32 v14, v14, v14\n"
        "v_bfi_b32 v35, s58, v14, v24\n"
        "v_bfi_b32 v37, s58, v14, v24\n"
        "v_permlane32_swap_b32 v34, v36\n"
        "s_nop 0\n"
        "v_permlane32_swap_b32 v35, v37\n"
        "s_mov_b64 exec, 1\n"
        "global_store_dwordx4 v3, v[34:37], s[28:29] sc1\n"
        "s_mov_b64 exec, -1\n"
        "s_cmp_ge_u32 s32, 10\n"
        "s_barrier\n"
        "s_cbranch_scc1 L_FFN\n"
        "s_mov_b64 exec, 0xffff\n"
        "global_load_dword v251, v41, s[50:51]\n"
        "s_mov_b32 exec_lo, s62\n"
        "s_cbranch_execz L_AEND\n"
        "global_load_dword v249, v42, s[24:25]\n"
        "L_AEND:\n"
        "s_endpgm\n"
        "L_FFN:\n"
        "s_cmp_ge_u32 s54, 43\n"
        "s_cbranch_scc1 L_SPARE\n"
        "s_cmp_lt_u32 s30, 2\n"
        "s_cbranch_scc1 L_F1\n"
        "s_barrier\n"
        "s_cmp_lt_u32 s30, 4\n"
        "s_cbranch_scc1 L_F1\n"
        "s_barrier\n"
        "L_F1:\n"
        "s_cmp_eq_u32 s30, 0\n"
        "s_cbranch_scc1 L_F2\n"
        "s_add_u32 m0, s63, 0\n"
        "s_nop 0\n"
        "global_load_lds_dwordx4 v44, s[20:21]\n"
        "s_add_u32 m0, s63, 4096\n"
        "s_nop 0\n"
        "global_load_lds_dwordx4 v44, s[22:23]\n"
        "s_add_u32 m0, s63, 1024\n"
        "s_nop 0\n"
        "global_load_lds_dwordx4 v45, s[20:21]\n"
        "s_add_u32 m0, s63, 5120\n"
        "s_nop 0\n"
        "global_load_lds_dwordx4 v45, s[22:23]\n"
        "s_add_u32 m0, s63, 2048\n"
        "s_nop 0\n"
        "global_load_lds_dwordx4 v46, s[20:21]\n"
        "s_add_u32 m0, s63, 6144\n"
        "s_nop 0\n"
        "global_load_lds_dwordx4 v46, s[22:23]\n"
        "s_add_u32 m0, s63, 3072\n"
        "s_nop 0\n"
        "global_load_lds_dwordx4 v47, s[20:21]\n"
        "s_add_u32 m0, s63, 7168\n"
        "s_nop 0\n"
        "global_load_lds_dwordx4 v47, s[22:23]\n"
        "L_F2:\n"
        "s_cmp_ge_u32 s30, 4\n"
        "s_cbranch_scc1 L_F3\n"
        "s_barrier\n"
        "s_cmp_ge_u32 s30, 2\n"
        "s_cbranch_scc1 L_F3\n"
        "s_barrier\n"
        "L_F3:\n"
        "s_add_u32 s48, s28, 4096\n"
        "s_addc_u32 s49, s29, 0\n"
        "s_add_u32 s50, s28, 12288\n"
        "s_addc_u32 s51, s29, 0\n"
        "v_lshlrev_b32 v26, 4, v0\n"
        "s_lshl_b32 s38, s54, 15\n"
        "s_lshl_b32 s39, s30, 7\n"
        "s_add_u32 s38, s38, s39\n"
        "v_and_b32 v18, 16, v1\n"
        "v_lshlrev_b32 v18, 1, v18\n"
        "v_xor_b32_e64 v5, v18, v8\n"
        "v_and_b32 v5, 0x70, v5\n"
        "v_lshrrev_b32_e64 v3, 3, v1\n"
        "v_lshl_add_u32 v3, v3, 10, v5\n"
        "v_add_u32_e64 v14, s38, v3\n"
        "v_xor_b32 v15, 64, v14\n"
        "v_add_u32 v15, 0x2000, v15\n"
        "v_add_u32 v16, 0x4000, v14\n"
        "v_add_u32 v17, 0x4000, v15\n"
        "s_barrier\n"
        "global_load_dwordx4 v[152:155], v26, s[48:49] sc1\n"
        "global_load_dwordx4 v[156:159], v26, s[50:51] sc1\n"
        "s_add_u32 m0, s63, 8192\n"
        "s_nop 0\n"
        "global_load_lds_dwordx4 v14, s[24:25]\n"
        "s_add_u32 m0, s63, 9216\n"
        "s_nop 0\n"
        "global_load_lds_dwordx4 v15, s[24:25]\n"
        "s_add_u32 m0, s63, 10240\n"
        "s_nop 0\n"
        "global_load_lds_dwordx4 v16, s[24:25]\n"
        "s_add_u32 m0, s63, 11264\n"
        "s_nop 0\n"
        "global_load_lds_dwordx4 v17, s[24:25]\n"
        "v_and_b32 v2, 15, v1\n"
        "s_lshl_b32 s38, s30, 9\n"
        "s_add_u32 s38, s38, 4096\n"
        "v_lshl_add_u32 v25, v2, 2, s38\n"
        "v_and_b32 v3, 48, v1\n"
        "v_lshrrev_b32 v9, 2, v2\n"
        "v_lshrrev_b32 v19, 1, v3\n"
        "v_add_u32 v9, v9, v19\n"
        "v_and_b32 v19, 3, v1\n"
        "v_lshl_add_u32 v19, v19, 3, s63\n"
        "v_lshl_add_u32 v204, v9, 6, v19\n"
        "v_add_u32 v204, v204, v18\n"
        "v_xor_b32 v205, 32, v204\n"
        "v_lshl_add_u32 v206, v9, 7, v19\n"
        "v_and_b32 v18, 24, v1\n"
        "v_lshl_add_u32 v206, v18, 2, v206\n"
        "v_xor_b32 v207, 32, v206\n"
        "v_xor_b32 v208, 64, v206\n"
        "v_xor_b32 v209, 64, v207\n"
        "s_lshl_b32 s38, s55, 7\n"
        "v_add_u32 v27, s38, v3\n"
        "s_mov_b32 s56, 0xaaaaaaaa\n"
        "s_mov_b32 s57, s56\n"
        "s_mov_b32 s60, 0\n"
        "s_mov_b32 s61, -1\n"
        "v_lshlrev_b32 v34, 1, v0\n"
        "v_or_b32 v35, 1, v34\n"
        "v_or_b32 v36, 0x400, v34\n"
        "v_or_b32 v37, 0x401, v34\n"
        "s_mul_i32 s38, s30, 288\n"
        "s_add_u32 s38, s38, 8448\n"
        "v_lshl_add_u32 v28, v1, 1, s38\n"
        "v_and_b32 v18, 1, v1\n"
        "v_lshl_add_u32 v18, v18, 6, v3\n"
        "v_add_u32 v18, s38, v18\n"
        "s_add_u32 s38, s38, 128\n"
        "v_mov_b32 v30, s38\n"
        "v_mov_b32 v31, s38\n"
        "v_mov_b32 v32, s38\n"
        "v_mov_b32 v33, s38\n"
        "v_mov_b32 v20, 0\n"
        "v_mov_b32 v21, 0\n"
        "ds_write2_b64 v30, v[20:21], v[20:21] offset1:1\n"
        "s_mov_b32 exec_lo, 0x30003\n"
        "s_mov_b32 exec_hi, 0x30003\n"
        "v_mov_b32_e64 v30, v18\n"
        "s_mov_b32 exec_lo, 0x300030\n"
        "s_mov_b32 exec_hi, 0x300030\n"
        "v_mov_b32_e64 v31, v18\n"
        "s_mov_b32 exec_lo, 0x3000300\n"
        "s_mov_b32 exec_hi, 0x3000300\n"
        "v_mov_b32 v32, v18\n"
        "s_mov_b32 exec_lo, 0x30003000\n"
        "s_mov_b32 exec_hi, 0x30003000\n"
        "v_mov_b32 v33, v18\n"
        "s_mov_b64 exec, -1\n"
        "s_lshl_b32 s38, s54, 12\n"
        "s_add_u32 s38, s38, 131072\n"
        "v_lshl_add_u32 v29, v0, 3, s38\n"
        "s_lshl_b32 s38, s30, 3\n"
        "v_mov_b32 v19, s38\n"
        "v_and_b32 v20, 7, v1\n"
        "v_lshlrev_b32 v20, 3, v20\n"
        "v_lshlrev_b32 v21, 2, v1\n"
        "s_and_b32 s38, s46, 0xff\n"
        "v_mov_b32 v38, s38\n"
        "s_bfe_u32 s38, s46, 0x80008\n"
        "v_mov_b32 v39, s38\n"
        "s_mov_b32 s47, 32768\n"
        "s_mov_b32 s59, 0xffffff00\n"
        "v_mov_b32 v5, v4\n"
        "v_mov_b64 v[226:227], v[4:5]\n"
        "v_mov_b64 v[228:229], v[4:5]\n"
        "v_mov_b64 v[230:231], v[4:5]\n"
        "v_mov_b64 v[232:233], v[4:5]\n"
        "s_waitcnt vmcnt(4)\n"
        "L_ACHK:\n"
        "v_xor_b32 v160, s45, v153\n"
        "v_bitop3_b32 v160, v155, s45, v160 bitop3:0xbe\n"
        "v_bitop3_b32 v160, v157, s45, v160 bitop3:0xbe\n"
        "v_bitop3_b32 v160, v159, s45, v160 bitop3:0xbe\n"
        "v_cmp_lt_u32_e64 vcc, s58, v160\n"
        "s_sub_u32 s47, s47, 1\n"
        "s_cbranch_vccnz L_ARETRY\n"
        "L_AOK:\n"
        "v_lshl_or_b32 v164, v153, 16, v34\n"
        "v_lshl_or_b32 v165, v155, 16, v35\n"
        "v_lshl_or_b32 v166, v157, 16, v36\n"
        "v_lshl_or_b32 v167, v159, 16, v37\n"
        "v_max_f32_e64 v160, v164, v165\n"
        "v_min_f32_e64 v161, v164, v165\n"
        "v_med3_f32 v161, v160, v161, v166\n"
        "v_max_f32_e64 v160, v160, v166\n"
        "v_med3_f32 v161, v160, v161, v167\n"
        "v_max_f32_e64 v160, v160, v167\n"
        "v_lshrrev_b32_e64 v162, 1, v26\n"
        "v_mov_b32_e64 v153, v154\n"
        "v_mov_b32_e64 v157, v158\n"
        "ds_write2st64_b64 v162, v[152:153], v[156:157] offset0:21 offset1:29\n"
        "v_min_f32_dpp v162, v160, v160 row_ror:1 row_mask:0xf bank_mask:0xf\n"
        "v_max_f32_dpp v160, v160, v160 row_ror:1 row_mask:0xf bank_mask:0xf\n"
        "v_max_f32_dpp v161, v161, v161 row_ror:1 row_mask:0xf bank_mask:0xf\n"
        "v_max_f32_e64 v161, v161, v162\n"
        "v_min_f32_dpp v162, v160, v160 row_ror:2 row_mask:0xf bank_mask:0xf\n"
        "v_max_f32_dpp v160, v160, v160 row_ror:2 row_mask:0xf bank_mask:0xf\n"
        "v_max_f32_dpp v161, v161, v161 row_ror:2 row_mask:0xf bank_mask:0xf\n"
        "v_max_f32_e64 v161, v161, v162\n"
        "v_min_f32_dpp v162, v160, v160 row_ror:4 row_mask:0xf bank_mask:0xf\n"
        "v_max_f32_dpp v160, v160, v160 row_ror:4 row_mask:0xf bank_mask:0xf\n"
        "v_max_f32_dpp v161, v161, v161 row_ror:4 row_mask:0xf bank_mask:0xf\n"
        "v_max_f32_e64 v161, v161, v162\n"
        "v_min_f32_dpp v162, v160, v160 row_ror:8 row_mask:0xf bank_mask:0xf\n"
        "v_max_f32_dpp v160, v160, v160 row_ror:8 row_mask:0xf bank_mask:0xf\n"
        "v_max_f32_dpp v161, v161, v161 row_ror:8 row_mask:0xf bank_mask:0xf\n"
        "v_max_f32_e64 v161, v161, v162\n"
        "v_min_f32_dpp v162, v160, v160 row_bcast:15 row_mask:0xa bank_mask:0xf\n"
        "v_max_f32_dpp v160, v160, v160 row_bcast:15 row_mask:0xa bank_mask:0xf\n"
        "v_max_f32_dpp v161, v161, v161 row_bcast:15 row_mask:0xa bank_mask:0xf\n"
        "v_max_f32_e64 v161, v161, v162\n"
        "v_min_f32_dpp v162, v160, v160 row_bcast:31 row_mask:0xc bank_mask:0xf\n"
        "v_max_f32_dpp v160, v160, v160 row_bcast:31 row_mask:0xc bank_mask:0xf\n"
        "v_max_f32_dpp v161, v161, v161 row_bcast:31 row_mask:0xc bank_mask:0xf\n"
        "v_max_f32_e64 v161, v161, v162\n"
        "s_mov_b32 exec_lo, 0\n"
        "s_brev_b32 exec_hi, 1\n"
        "ds_write_b64 v19, v[160:161] offset:8192\n"
        "s_mov_b64 exec, -1\n"
        "s_waitcnt lgkmcnt(0)\n"
        "s_barrier\n"
        "ds_read_b64 v[160:161], v20 offset:8192\n"
        "s_waitcnt lgkmcnt(0)\n"
        "v_min_f32_dpp v162, v160, v160 row_ror:1 row_mask:0xf bank_mask:0xf\n"
        "v_max_f32_dpp v160, v160, v160 row_ror:1 row_mask:0xf bank_mask:0xf\n"
        "v_max_f32_dpp v161, v161, v161 row_ror:1 row_mask:0xf bank_mask:0xf\n"
        "v_max_f32_e64 v161, v161, v162\n"
        "v_min_f32_dpp v162, v160, v160 row_ror:2 row_mask:0xf bank_mask:0xf\n"
        "v_max_f32_dpp v160, v160, v160 row_ror:2 row_mask:0xf bank_mask:0xf\n"
        "v_max_f32_dpp v161, v161, v161 row_ror:2 row_mask:0xf bank_mask:0xf\n"
        "v_max_f32_e64 v161, v161, v162\n"
        "v_min_f32_dpp v162, v160, v160 row_ror:4 row_mask:0xf bank_mask:0xf\n"
        "v_max_f32_dpp v160, v160, v160 row_ror:4 row_mask:0xf bank_mask:0xf\n"
        "v_max_f32_dpp v161, v161, v161 row_ror:4 row_mask:0xf bank_mask:0xf\n"
        "v_max_f32_e64 v161, v161, v162\n"
        "v_cndmask_b32 v164, v160, v161, s[56:57]\n"
        "v_and_b32 v164, 0x7ff, v164\n"
        "v_lshl_add_u32 v164, v164, 10, v27\n"
        "s_mov_b32 exec_lo, 0x00030003\n"
        "s_mov_b32 exec_hi, 0x00030003\n"
        "global_load_dwordx4 v[226:229], v164, s[4:5]\n"
        "global_load_dwordx4 v[230:233], v164, s[4:5] offset:64\n"
        "s_mov_b64 exec, -1\n"
        "v_cndmask_b32 v184, v160, v161, s[60:61]\n"
        "v_and_b32 v162, 0x7ff, v184\n"
        "v_lshlrev_b32 v162, 2, v162\n"
        "ds_read_b32 v186, v162 offset:10752\n"
        "ds_read_b64_tr_b16 v[88:89], v204 offset:0\n"
        "ds_read_b64_tr_b16 v[90:91], v204 offset:256\n"
        "ds_read_b64_tr_b16 v[92:93], v204 offset:2048\n"
        "ds_read_b64_tr_b16 v[94:95], v204 offset:2304\n"
        "ds_read_b64_tr_b16 v[96:97], v205 offset:0\n"
        "ds_read_b64_tr_b16 v[98:99], v205 offset:256\n"
        "ds_read_b64_tr_b16 v[100:101], v205 offset:2048\n"
        "ds_read_b64_tr_b16 v[102:103], v205 offset:2304\n"
        "ds_read_b64_tr_b16 v[104:105], v204 offset:4096\n"
        "ds_read_b64_tr_b16 v[106:107], v204 offset:4352\n"
        "ds_read_b64_tr_b16 v[108:109], v204 offset:6144\n"
        "ds_read_b64_tr_b16 v[110:111], v204 offset:6400\n"
        "ds_read_b64_tr_b16 v[112:113], v205 offset:4096\n"
        "ds_read_b64_tr_b16 v[114:115], v205 offset:4352\n"
        "ds_read_b64_tr_b16 v[116:117], v205 offset:6144\n"
        "ds_read_b64_tr_b16 v[118:119], v205 offset:6400\n"
        "s_waitcnt vmcnt(2)\n"
        "ds_read_b64_tr_b16 v[120:121], v206 offset:8192\n"
        "ds_read_b64_tr_b16 v[122:123], v206 offset:8704\n"
        "s_nop 0\n"
        "ds_read_b64_tr_b16 v[124:125], v207 offset:8192\n"
        "ds_read_b64_tr_b16 v[126:127], v207 offset:8704\n"
        "ds_read_b64_tr_b16 v[128:129], v208 offset:8192\n"
        "ds_read_b64_tr_b16 v[130:131], v208 offset:8704\n"
        "ds_read_b64_tr_b16 v[132:133], v209 offset:8192\n"
        "ds_read_b64_tr_b16 v[134:135], v209 offset:8704\n"
        "s_waitcnt lgkmcnt(14)\n"
        "v_and_b32 v166, 0xffff0000, v186\n"
        "v_readlane_b32 s38, v186, 0\n"
        "v_readlane_b32 s39, v186, 32\n"
        "v_rcp_f32 v166, v166\n"
        "s_lshl_b32 s60, s38, 16\n"
        "s_lshl_b32 s61, s39, 16\n"
        "s_cmp_lg_u32 s30, 7\n"
        "s_cbranch_scc1 L_ANOH\n"
        "s_lshl_b32 s38, s54, 7\n"
        "s_add_u32 s38, s38, 65536\n"
        "v_lshrrev_b32 v3, 1, v1\n"
        "v_add_u32_e64 v3, s38, v3\n"
        "v_mov_b32_e64 v185, s45\n"
        "v_mov_b32 v187, s45\n"
        "s_mov_b32 exec_lo, 1\n"
        "s_mov_b32 exec_hi, 1\n"
        "global_store_dwordx4 v3, v[184:187], s[28:29] sc1\n"
        "s_mov_b64 exec, -1\n"
        "s_waitcnt vmcnt(1)\n"
        "s_branch L_AH2\n"
        "L_ANOH:\n"
        "s_waitcnt vmcnt(0)\n"
        "L_AH2:\n"
        "s_waitcnt lgkmcnt(0)\n"
        "v_mfma_f32_16x16x32_bf16 v[136:139], v[226:229], v[88:91], 0\n"
        "v_mfma_f32_16x16x32_bf16 v[136:139], v[230:233], v[92:95], v[136:139]\n"
        "v_mfma_f32_16x16x32_bf16 v[140:143], v[226:229], v[96:99], 0\n"
        "v_mfma_f32_16x16x32_bf16 v[140:143], v[230:233], v[100:103], v[140:143]\n"
        "v_mfma_f32_16x16x32_bf16 v[144:147], v[226:229], v[104:107], 0\n"
        "v_mfma_f32_16x16x32_bf16 v[144:147], v[230:233], v[108:111], v[144:147]\n"
        "v_mfma_f32_16x16x32_bf16 v[148:151], v[226:229], v[112:115], 0\n"
        "v_mfma_f32_16x16x32_bf16 v[148:151], v[230:233], v[116:119], v[148:151]\n"
        "s_mov_b64 exec, 0xffff\n"
        "s_nop 0\n"
        "v_fma_f32 v136, -v138, s60, v136\n"
        "s_nop 0\n"
        "v_fma_f32 v137, -v138, s61, v137\n"
        "v_fma_f32 v140, -v142, s60, v140\n"
        "v_fma_f32 v141, -v142, s61, v141\n"
        "v_fma_f32 v144, -v146, s60, v144\n"
        "v_fma_f32 v145, -v146, s61, v145\n"
        "v_fma_f32 v148, -v150, s60, v148\n"
        "v_fma_f32 v149, -v150, s61, v149\n"
        "ds_write2_b32 v25, v136, v137 offset0:0 offset1:32\n"
        "ds_write2_b32 v25, v140, v141 offset0:16 offset1:48\n"
        "ds_write2_b32 v25, v144, v145 offset0:64 offset1:96\n"
        "ds_write2_b32 v25, v148, v149 offset0:80 offset1:112\n"
        "s_mov_b64 exec, -1\n"
        "s_waitcnt lgkmcnt(0)\n"
        "s_barrier\n"
        "ds_read2st64_b32 v[168:169], v21 offset0:16 offset1:17\n"
        "ds_read2st64_b32 v[170:171], v21 offset0:18 offset1:19\n"
        "ds_read2st64_b32 v[172:173], v21 offset0:20 offset1:21\n"
        "s_nop 0\n"
        "ds_read2st64_b32 v[174:175], v21 offset0:22 offset1:23\n"
        "ds_read2st64_b32 v[176:177], v21 offset0:24 offset1:25\n"
        "ds_read2st64_b32 v[178:179], v21 offset0:26 offset1:27\n"
        "ds_read2st64_b32 v[180:181], v21 offset0:28 offset1:29\n"
        "ds_read2st64_b32 v[182:183], v21 offset0:30 offset1:31\n"
        "s_waitcnt lgkmcnt(6)\n"
        "v_pk_add_f32 v[168:169], v[168:169], v[170:171]\n"
        "s_waitcnt lgkmcnt(4)\n"
        "v_pk_add_f32 v[172:173], v[172:173], v[174:175]\n"
        "s_waitcnt lgkmcnt(2)\n"
        "v_pk_add_f32 v[176:177], v[176:177], v[178:179]\n"
        "s_waitcnt lgkmcnt(0)\n"
        "v_pk_add_f32 v[180:181], v[180:181], v[182:183]\n"
        "v_pk_add_f32 v[168:169], v[168:169], v[172:173]\n"
        "v_pk_add_f32 v[176:177], v[176:177], v[180:181]\n"
        "v_pk_add_f32 v[168:169], v[168:169], v[176:177]\n"
        "v_pk_mul_f32 v[168:169], v[168:169], v[166:167] op_sel_hi:[1,0]\n"
        "v_cvt_pk_bf16_f32 v169, v168, v169\n"
        "v_lshlrev_b32 v168, 16, v169\n"
        "v_mul_f32 v170, 0xbfb8aa3b, v168\n"
        "v_exp_f32 v170, v170\n"
        "v_and_b32 v169, 0xffff0000, v169\n"
        "v_add_f32_e64 v170, 1.0, v170\n"
        "v_rcp_f32 v170, v170\n"
        "s_nop 0\n"
        "v_mul_f32_e64 v168, v168, v170\n"
        "v_cvt_pk_bf16_f32 v168, v168, v168\n"
        "v_lshlrev_b32_e64 v168, 16, v168\n"
        "v_mul_f32_e64 v168, v168, v169\n"
        "v_cvt_pk_bf16_f32 v168, v168, v168\n"
        "ds_write_b16 v28, v168\n"
        "ds_read_b128 v[188:191], v30\n"
        "ds_read_b128 v[192:195], v31\n"
        "ds_read_b128 v[196:199], v32\n"
        "ds_read_b128 v[200:203], v33\n"
        "s_waitcnt lgkmcnt(3)\n"
        "v_mfma_f32_16x16x32_bf16 v[136:139], v[188:191], v[120:123], 0\n"
        "s_waitcnt lgkmcnt(2)\n"
        "v_mfma_f32_16x16x32_bf16 v[136:139], v[192:195], v[124:127], v[136:139]\n"
        "s_waitcnt lgkmcnt(1)\n"
        "v_mfma_f32_16x16x32_bf16 v[136:139], v[196:199], v[128:131], v[136:139]\n"
        "s_waitcnt lgkmcnt(0)\n"
        "v_mfma_f32_16x16x32_bf16 v[136:139], v[200:203], v[132:135], v[136:139]\n"
        "s_nop 7\n"
        "v_bfi_b32 v136, s59, v136, v38\n"
        "v_bfi_b32 v137, s59, v137, v39\n"
        "global_store_dwordx2 v29, v[136:137], s[28:29] sc1\n"
        "s_endpgm\n"
        "L_ARETRY:\n"
        "s_cbranch_scc1 L_AOK\n"
        "global_load_dwordx4 v[152:155], v26, s[48:49] sc1\n"
        "global_load_dwordx4 v[156:159], v26, s[50:51] sc1\n"
        "s_waitcnt vmcnt(0)\n"
        "s_branch L_ACHK\n"
        "L_SPARE:\n"
        "s_cmp_lg_u32 s35, 120\n"
        "s_cbranch_scc1 L_SEND\n"
        "v_lshlrev_b32 v3, 7, v1\n"
        "v_add_u32 v4, 8192, v8\n"
        "v_add_u32 v5, 8192, v4\n"
        "v_add_u32 v6, 32768, v3\n"
        "v_add_u32 v7, 40960, v3\n"
        "s_mov_b32 s47, 32768\n"
        "s_not_b32 s38, s46\n"
        "s_sleep 50\n"
        "L_UPOLL:\n"
        "s_sleep 4\n"
        "global_load_dwordx4 v[60:63], v4, s[28:29] offset:-4096 sc1\n"
        "global_load_dwordx4 v[64:67], v4, s[28:29] offset:-3072 sc1\n"
        "global_load_dwordx4 v[68:71], v4, s[28:29] offset:-2048 sc1\n"
        "global_load_dwordx4 v[72:75], v4, s[28:29] offset:-1024 sc1\n"
        "global_load_dwordx4 v[76:79], v4, s[28:29] offset:0 sc1\n"
        "global_load_dwordx4 v[80:83], v4, s[28:29] offset:1024 sc1\n"
        "global_load_dwordx4 v[84:87], v4, s[28:29] offset:2048 sc1\n"
        "global_load_dwordx4 v[88:91], v4, s[28:29] offset:3072 sc1\n"
        "global_load_dwordx4 v[92:95], v5, s[28:29] offset:-4096 sc1\n"
        "global_load_dwordx4 v[96:99], v5, s[28:29] offset:-3072 sc1\n"
        "global_load_dwordx4 v[100:103], v5, s[28:29] offset:-2048 sc1\n"
        "global_load_dwordx4 v[104:107], v5, s[28:29] offset:-1024 sc1\n"
        "global_load_dwordx4 v[108:111], v5, s[28:29] offset:0 sc1\n"
        "global_load_dwordx4 v[112:115], v5, s[28:29] offset:1024 sc1\n"
        "global_load_dwordx4 v[116:119], v5, s[28:29] offset:2048 sc1\n"
        "global_load_dwordx4 v[120:123], v5, s[28:29] offset:3072 sc1\n"
        "global_load_dwordx2 v[48:49], v6, s[28:29] sc1\n"
        "global_load_dwordx2 v[50:51], v7, s[28:29] sc1\n"
        "s_waitcnt vmcnt(0)\n"
        "v_xor_b32 v52, s45, v61\n"
        "v_bitop3_b32 v52, v63, s45, v52 bitop3:0xbe\n"
        "v_bitop3_b32 v52, v65, s45, v52 bitop3:0xbe\n"
        "v_bitop3_b32 v52, v67, s45, v52 bitop3:0xbe\n"
        "v_bitop3_b32 v52, v69, s45, v52 bitop3:0xbe\n"
        "v_bitop3_b32 v52, v71, s45, v52 bitop3:0xbe\n"
        "v_bitop3_b32 v52, v73, s45, v52 bitop3:0xbe\n"
        "v_bitop3_b32 v52, v75, s45, v52 bitop3:0xbe\n"
        "v_bitop3_b32 v52, v77, s45, v52 bitop3:0xbe\n"
        "v_bitop3_b32 v52, v79, s45, v52 bitop3:0xbe\n"
        "v_bitop3_b32 v52, v81, s45, v52 bitop3:0xbe\n"
        "v_bitop3_b32 v52, v83, s45, v52 bitop3:0xbe\n"
        "v_bitop3_b32 v52, v85, s45, v52 bitop3:0xbe\n"
        "v_bitop3_b32 v52, v87, s45, v52 bitop3:0xbe\n"
        "v_bitop3_b32 v52, v89, s45, v52 bitop3:0xbe\n"
        "v_bitop3_b32 v52, v91, s45, v52 bitop3:0xbe\n"
        "v_bitop3_b32 v52, v93, s45, v52 bitop3:0xbe\n"
        "v_bitop3_b32 v52, v95, s45, v52 bitop3:0xbe\n"
        "v_bitop3_b32 v52, v97, s45, v52 bitop3:0xbe\n"
        "v_bitop3_b32 v52, v99, s45, v52 bitop3:0xbe\n"
        "v_bitop3_b32 v52, v101, s45, v52 bitop3:0xbe\n"
        "v_bitop3_b32 v52, v103, s45, v52 bitop3:0xbe\n"
        "v_bitop3_b32 v52, v105, s45, v52 bitop3:0xbe\n"
        "v_bitop3_b32 v52, v107, s45, v52 bitop3:0xbe\n"
        "v_bitop3_b32 v52, v109, s45, v52 bitop3:0xbe\n"
        "v_bitop3_b32 v52, v111, s45, v52 bitop3:0xbe\n"
        "v_bitop3_b32 v52, v113, s45, v52 bitop3:0xbe\n"
        "v_bitop3_b32 v52, v115, s45, v52 bitop3:0xbe\n"
        "v_bitop3_b32 v52, v117, s45, v52 bitop3:0xbe\n"
        "v_bitop3_b32 v52, v119, s45, v52 bitop3:0xbe\n"
        "v_bitop3_b32 v52, v121, s45, v52 bitop3:0xbe\n"
        "v_bitop3_b32 v52, v123, s45, v52 bitop3:0xbe\n"
        "v_lshrrev_b32_e64 v52, 16, v52\n"
        "v_bitop3_b32 v52, v48, s46, v52 bitop3:0xbe\n"
        "v_bitop3_b32 v52, v49, s38, v52 bitop3:0xbe\n"
        "v_bitop3_b32 v52, v50, s46, v52 bitop3:0xbe\n"
        "v_bitop3_b32 v52, v51, s38, v52 bitop3:0xbe\n"
        "v_cmp_ne_u32 vcc, 0, v52\n"
        "s_sub_u32 s47, s47, 1\n"
        "s_cbranch_vccz L_UOK\n"
        "s_cbranch_scc0 L_UPOLL\n"
        "L_UOK:\n"
        "v_mov_b32 v3, 0\n"
        "v_mov_b32_e64 v4, s46\n"
        "s_mov_b64 exec, 1\n"
        "global_store_dword v3, v4, s[28:29] sc1\n"
        "L_SEND:\n"
        "s_endpgm\n"
        ".fill 565, 4, 0xbf800000\n"
        :
        : "{s[0:1]}"(__builtin_amdgcn_kernarg_segment_ptr()), "{s2}"(__builtin_amdgcn_workgroup_id_x()), "{v0}"(__builtin_amdgcn_workitem_id_x())
        : "memory", "scc", "vcc", "s3", "s4", "s5", "s6", "s7", "s8", "s9", "s10", "s11", "s12", "s13", "s14", "s15", "s16", "s17", "s18", "s19", "s20", "s21", "s22", "s23", "s24", "s25", "s26", "s27", "s28", "s29", "s30", "s31", "s33", "s34", "s35", "s36", "s37", "s38", "s39", "s40", "s41", "s42", "s43", "s44", "s45", "s46", "s47", "s48", "s49", "s50", "s51", "s52", "s53", "s54", "s55", "s56", "s57", "s58", "s59", "s60", "s61", "s62", "s63", "s64", "s65", "s66", "s67", "s68", "s69", "s70", "s71", "s72", "s73", "s74", "s75", "s76", "s77", "s78", "s79", "s80", "s81", "s82", "s83", "s84", "s85", "s86", "s87", "s88", "s89", "s90", "s91", "s92", "s93", "s94", "s95", "v1", "v2", "v3", "v4", "v5", "v6", "v7", "v8", "v9", "v10", "v11", "v12", "v13", "v14", "v15", "v16", "v17", "v18", "v19", "v20", "v21", "v22", "v23", "v24", "v25", "v26", "v27", "v28", "v29", "v30", "v31", "v32", "v33", "v34", "v35", "v36", "v37", "v38", "v39", "v40", "v41", "v42", "v43", "v44", "v45", "v46", "v47", "v48", "v49", "v50", "v51", "v52", "v53", "v54", "v55", "v56", "v57", "v58", "v59", "v60", "v61", "v62", "v63", "v64", "v65", "v66", "v67", "v68", "v69", "v70", "v71", "v72", "v73", "v74", "v75", "v76", "v77", "v78", "v79", "v80", "v81", "v82", "v83", "v84", "v85", "v86", "v87", "v88", "v89", "v90", "v91", "v92", "v93", "v94", "v95", "v96", "v97", "v98", "v99", "v100", "v101", "v102", "v103", "v104", "v105", "v106", "v107", "v108", "v109", "v110", "v111", "v112", "v113", "v114", "v115", "v116", "v117", "v118", "v119", "v120", "v121", "v122", "v123", "v124", "v125", "v126", "v127", "v128", "v129", "v130", "v131", "v132", "v133", "v134", "v135", "v136", "v137", "v138", "v139", "v140", "v141", "v142", "v143", "v144", "v145", "v146", "v147", "v148", "v149", "v150", "v151", "v152", "v153", "v154", "v155", "v156", "v157", "v158", "v159", "v160", "v161", "v162", "v163", "v164", "v165", "v166", "v167", "v168", "v169", "v170", "v171", "v172", "v173", "v174", "v175", "v176", "v177", "v178", "v179", "v180", "v181", "v182", "v183", "v184", "v185", "v186", "v187", "v188", "v189", "v190", "v191", "v192", "v193", "v194", "v195", "v196", "v197", "v198", "v199", "v200", "v201", "v202", "v203", "v204", "v205", "v206", "v207", "v208", "v209", "v210", "v211", "v212", "v213", "v214", "v215", "v216", "v217", "v218", "v219", "v220", "v221", "v222", "v223", "v224", "v225", "v226", "v227", "v228", "v229", "v230", "v231", "v232", "v233", "v234", "v235", "v236", "v237", "v238", "v239", "v240", "v241", "v242", "v243", "v244", "v245", "v246", "v247", "v248", "v249", "v250", "v251", "v252", "v253", "v254", "v255");
#endif
}

extern "C" void kernel_launch(void* const* d_in, const int* in_sizes, int n_in, void* d_out, int out_size,
                              void* d_ws, size_t ws_size, hipStream_t stream) {
    (void)hipFuncSetAttribute((const void*)s4d_moe_kernel, hipFuncAttributeMaxDynamicSharedMemorySize, 125440);
    s4d_moe_kernel<<<256, 512, 125440, stream>>>(d_in[0], d_in[1], d_in[2], d_in[3], d_in[4], d_in[5], d_in[6], d_in[8],
                                               d_in[9], d_in[10], d_in[11], d_out, d_ws);
}
```
